```python
import torch

def _softmax(x, axis=-1):
    return torch.softmax(x, dim=axis)
def _gelu(x):
    return torch.nn.functional.gelu(x, approximate="tanh")
def _silu(x):
    return torch.nn.functional.silu(x)
def _sigmoid(x):
    return torch.sigmoid(x)
def _layernorm(x, axis=-1, eps=1e-5):
    m = x.mean(dim=axis, keepdim=True)
    v = x.var(dim=axis, keepdim=True, unbiased=False)
    return (x - m) / torch.sqrt(v + eps)
def _scatter_add(tgt, idx, src, axis=0):
    return tgt.clone().index_add_(axis, idx, src)
def _segment_sum(src, seg, n):
    out = torch.zeros((n,) + src.shape[1:], dtype=src.dtype)
    return out.index_add_(0, seg, src)
def _pad(x, pw):
    flat = tuple(v for pair in reversed(pw) for v in pair)
    return torch.nn.functional.pad(x, flat)

kernel_name = 'parallel_sum__ssm_conv_h3__glu_gated_bilinear__prompt'

def setup_inputs():

    _w = (lambda a: (lambda t: t.to(torch.bfloat16) if t.is_floating_point() else t)(torch.as_tensor(a)))
    def _A_setup():
        B, S, D = 1, 2048, 640
        ks = 4
        return {
            "q": _w((lambda *s: torch.randn(*s, dtype=torch.bfloat16) * 0.05)(B, S, D)),
            "k": _w((lambda *s: torch.randn(*s, dtype=torch.bfloat16) * 0.2)(B, S, D)),
            "v": _w((lambda *s: torch.randn(*s, dtype=torch.bfloat16) * 0.2)(B, S, D)),
            "shift_k": _w((lambda *s: torch.randn(*s, dtype=torch.bfloat16) * 0.2)(D, ks) * 0.5),
            "ssm_k": _w((lambda *s: torch.randn(*s, dtype=torch.bfloat16) * 0.4)(D, S) * (1.0 / S ** 0.5)),
            "ssm_D": _w((lambda *s: torch.randn(*s, dtype=torch.bfloat16) * 0.2)(D)),
        }
    _A_in = _A_setup()

    def _B_setup():
        B, S, D, F = 1, 2048, 640, 1728
        return {
            "x": _w((lambda *s: torch.randn(*s, dtype=torch.bfloat16) * 0.05)(B, S, D)),
            "w_gate": _w((lambda *s: torch.randn(*s, dtype=torch.bfloat16) * 0.2)(D, F) * (D ** -0.5)),
            "w_up": _w((lambda *s: torch.randn(*s, dtype=torch.bfloat16) * 0.2)(D, F) * (D ** -0.5)),
            "w_down": _w((lambda *s: torch.randn(*s, dtype=torch.bfloat16) * 0.2)(F, D) * (F ** -0.5)),
        }
    _B_in = _B_setup()

    return {
        "A_q": _A_in["q"],
        "A_k": _A_in["k"],
        "A_v": _A_in["v"],
        "A_shift_k": _A_in["shift_k"],
        "A_ssm_k": _A_in["ssm_k"],
        "A_ssm_D": _A_in["ssm_D"],
        "B_x": _B_in["x"],
        "B_w_gate": _B_in["w_gate"],
        "B_w_up": _B_in["w_up"],
        "B_w_down": _B_in["w_down"],
    }

def reference(A_q, A_k, A_v, A_shift_k, A_ssm_k, A_ssm_D, B_x, B_w_gate, B_w_up, B_w_down):
    def _A_ref(q, k, v, shift_k, ssm_k, ssm_D):
        B, S, D = q.shape
        ks = shift_k.shape[-1]
        k_pad = _pad(k, ((0, 0), (ks - 1, 0), (0, 0)))
        k_shift = torch.zeros_like(k)
        for t in range(ks):
            k_shift = k_shift + k_pad[:, t:t + S, :] * shift_k[:, ks - 1 - t][None, None, :]
        kv = k_shift * v
        L = 2 * S
        kv_pad = _pad(kv, ((0, 0), (0, S), (0, 0)))
        kf_pad = _pad(ssm_k, ((0, 0), (0, S)))
        kv_f = (lambda x, axes=(-2,-1): torch.fft.rfft2(x.float(), dim=axes))(kv_pad[:, None, :, :], axes=(1, 2))
        kf_f = (lambda x, axes=(-2,-1): torch.fft.rfft2(x.float(), dim=axes))(kf_pad[:, None, :], axes=(1, 2))
        kf_f = (lambda x, *d: x.permute(*d))(kf_f, 1, 2, 0)
        y_f = kv_f * kf_f[None, :, :, :]
        y = (lambda x, s, axes=(-2,-1): torch.fft.irfft2(x, s=s, dim=axes).to(torch.bfloat16))(y_f, (1, L), axes=(1, 2))[:, 0, :S, :]
        y = y + kv * ssm_D[None, None, :]
        return q * y

    def _B_ref(x, w_gate, w_up, w_down):
        g = torch.einsum("bsd,df->bsf", x, w_gate)
        u = torch.einsum("bsd,df->bsf", x, w_up)
        h = g * u
        return torch.einsum("bsf,fd->bsd", h, w_down)

    x = A_q
    xn = _layernorm(x, axis=-1)
    return x + _A_ref(xn, A_k, A_v, A_shift_k, A_ssm_k, A_ssm_D) + _B_ref(xn, B_w_gate, B_w_up, B_w_down)
```

```cpp
#include <hip/hip_runtime.h>
#include <hip/hip_bf16.h>
#include <stdint.h>

typedef unsigned short u16;
typedef __attribute__((ext_vector_type(8))) short bf16x8;
typedef __attribute__((ext_vector_type(4))) short s16x4;
typedef __attribute__((ext_vector_type(4))) float f32x4;
typedef __attribute__((ext_vector_type(2))) float f32x2;
typedef __attribute__((ext_vector_type(4))) unsigned u32x4;
typedef __attribute__((ext_vector_type(2))) unsigned u32x2;
typedef __attribute__((ext_vector_type(2))) __bf16 bf16x2_t;
#define LDS_AS __attribute__((address_space(3)))
#define GLB_AS __attribute__((address_space(1)))

constexpr int S_ = 2048, D_ = 640, F_ = 1728;

__device__ __forceinline__ float bflo(unsigned v) { return __uint_as_float(v << 16); }
__device__ __forceinline__ float bfhi(unsigned v) { return __uint_as_float(v & 0xffff0000u); }
__device__ __forceinline__ unsigned pack2(float a, float b) {
  f32x2 v = {a, b};
  bf16x2_t r = __builtin_convertvector(v, bf16x2_t);
  return __builtin_bit_cast(unsigned, r);
}
__device__ __forceinline__ float rbf(float a) { __bf16 r = (__bf16)a; return (float)r; }

__device__ __forceinline__ void glds16(unsigned voff, const void* sbase, unsigned lds_addr) {
  asm volatile("s_mov_b32 m0, %2\n\ts_nop 0\n\tglobal_load_lds_dwordx4 %0, %1" ::"v"(voff), "s"(sbase), "s"(lds_addr) : "memory");
}
__device__ __forceinline__ void glds16v(const void* vptr, unsigned lds_addr) {
  asm volatile("s_mov_b32 m0, %1\n\ts_nop 0\n\tglobal_load_lds_dwordx4 %0, off" ::"v"(vptr), "s"(lds_addr) : "memory");
}
__device__ __forceinline__ unsigned lds_off(const void* p) { return (unsigned)(uintptr_t)(const LDS_AS char*)p; }
__device__ __forceinline__ s16x4 trread(const char* lds) {
  return __builtin_amdgcn_ds_read_tr16_b64_v4i16((LDS_AS s16x4*)lds);
}
#define WAIT_V(n) asm volatile("s_waitcnt vmcnt(" #n ")" ::: "memory")
#define WAIT_L0() asm volatile("s_waitcnt lgkmcnt(0)" ::: "memory")

__device__ __forceinline__ float wave_sum(float v) {
#pragma unroll
  for (int o = 32; o >= 1; o >>= 1) v += __shfl_xor(v, o, 64);
  return v;
}

__global__ __launch_bounds__(256) void k_prep(const u16* __restrict__ x, const u16* __restrict__ k,
                                              const u16* __restrict__ v, const u16* __restrict__ sw,
                                              u16* __restrict__ xn, u16* __restrict__ uT) {
  __shared__ unsigned tile[64 * 33];
  const int b = blockIdx.x, tid = threadIdx.x, lane = tid & 63, w = tid >> 6;
  if (b < 256) {
#pragma unroll
    for (int rr = 0; rr < 2; rr++) {
      const int row = b * 8 + w * 2 + rr;
      const unsigned* xr = (const unsigned*)(x + row * D_);
      unsigned vals[5];
#pragma unroll
      for (int i = 0; i < 5; i++) vals[i] = xr[lane + 64 * i];
      float s = 0.f;
#pragma unroll
      for (int i = 0; i < 5; i++) s += bflo(vals[i]) + bfhi(vals[i]);
      s = wave_sum(s);
      const float mean = s * (1.0f / D_);
      float q = 0.f;
#pragma unroll
      for (int i = 0; i < 5; i++) {
        float a = bflo(vals[i]) - mean, c = bfhi(vals[i]) - mean;
        q += a * a + c * c;
      }
      q = wave_sum(q);
      const float var = q * (1.0f / D_);
      const float m = rbf(mean);
      const float sd = rbf(sqrtf(rbf(rbf(var) + 1e-5f)));
      unsigned* o = (unsigned*)(xn + row * D_);
#pragma unroll
      for (int i = 0; i < 5; i++)
        o[lane + 64 * i] = pack2(rbf(bflo(vals[i]) - m) / sd, rbf(bfhi(vals[i]) - m) / sd);
    }
  } else {
    const int bb = b - 256;
    const int t0 = (bb / 10) * 64, d0 = (bb % 10) * 64;
    const int dg = tid & 7, tp = tid >> 3;
    const int t = t0 + 2 * tp, d = d0 + 8 * dg;
    u32x4 kr[5], vr[2], wr[4];
#pragma unroll
    for (int i = 0; i < 5; i++) {
      int ti = t - 3 + i;
      kr[i] = (ti >= 0) ? *(const u32x4*)(k + ti * D_ + d) : (u32x4){0, 0, 0, 0};
    }
#pragma unroll
    for (int i = 0; i < 2; i++) vr[i] = *(const u32x4*)(v + (t + i) * D_ + d);
#pragma unroll
    for (int i = 0; i < 4; i++) wr[i] = *(const u32x4*)(sw + d * 4 + i * 8);
#pragma unroll
    for (int e = 0; e < 8; e++) {
      unsigned wa = wr[e >> 1][(e & 1) * 2], wb = wr[e >> 1][(e & 1) * 2 + 1];
      float w0 = bflo(wa), w1 = bfhi(wa), w2 = bflo(wb), w3 = bfhi(wb);
      float kk[5];
#pragma unroll
      for (int i = 0; i < 5; i++) kk[i] = (e & 1) ? bfhi(kr[i][e >> 1]) : bflo(kr[i][e >> 1]);
      float r[2];
#pragma unroll
      for (int i = 0; i < 2; i++) {
        float acc = rbf(kk[i] * w3);
        acc = rbf(acc + rbf(kk[i + 1] * w2));
        acc = rbf(acc + rbf(kk[i + 2] * w1));
        acc = rbf(acc + rbf(kk[i + 3] * w0));
        float vv = (e & 1) ? bfhi(vr[i][e >> 1]) : bflo(vr[i][e >> 1]);
        r[i] = acc * vv;
      }
      tile[(8 * dg + e) * 33 + tp] = pack2(r[0], r[1]);
    }
    __syncthreads();
    const int dd = tid >> 2, part = tid & 3;
    u32x4 o0, o1;
#pragma unroll
    for (int i = 0; i < 4; i++) { o0[i] = tile[dd * 33 + part * 8 + i]; o1[i] = tile[dd * 33 + part * 8 + 4 + i]; }
    u16* dst = uT + (d0 + dd) * S_ + t0 + part * 16;
    *(u32x4*)dst = o0;
    *(u32x4*)(dst + 8) = o1;
  }
}

constexpr int G1_A = 16384, G1_B = 28672, G1_STAGE = G1_A + G1_B, G1_NST = 3;
__global__ __launch_bounds__(256) void k_gemm1(const u16* __restrict__ xn, const u16* __restrict__ wg,
                                               const u16* __restrict__ wu, u16* __restrict__ h) {
  extern __shared__ __attribute__((aligned(16))) char smem[];
  const int tid = threadIdx.x, lane = tid & 63;
  const int w = __builtin_amdgcn_readfirstlane(tid >> 6);
  const int bx = blockIdx.x, xcd = bx & 7, idx = bx >> 3;
  const int mp = (xcd & 1) * 8 + (idx & 7), nw = (xcd >> 1) * 4 + (idx >> 3);
  const int m0 = mp * 128, n0 = (108 * nw) & ~7;

  unsigned asrc[4];
  const u16* bsrc[7];
#pragma unroll
  for (int i = 0; i < 4; i++) {
    int slot = (w * 4 + i) * 64 + lane, row = slot >> 3, chunk = (slot & 7) ^ ((row >> 1) & 7);
    asrc[i] = ((m0 + row) * D_ + chunk * 8) * 2;
  }
#pragma unroll
  for (int i = 0; i < 7; i++) {
    int slot = (w * 7 + i) * 64 + lane, kk = slot / 28, p = slot % 28;
    int mat = p & 1, tau = (p >> 1) ^ ((kk >> 3) & 1);
    bsrc[i] = (mat ? wu : wg) + (kk * F_ + n0 + 8 * tau);
  }
  const unsigned sm0 = lds_off(smem);
  auto issue = [&](int kt, int buf) {
    unsigned base = sm0 + buf * G1_STAGE;
#pragma unroll
    for (int i = 0; i < 4; i++) glds16(asrc[i], xn + kt * 64, base + (w * 4 + i) * 1024);
#pragma unroll
    for (int i = 0; i < 7; i++) glds16v(bsrc[i] + kt * 64 * F_, base + G1_A + (w * 7 + i) * 1024);
  };

  const int wr = w >> 1, wc = w & 1;
  const int g = lane >> 4, r16 = lane & 15, q = r16 >> 2, p = lane & 3;
  int aoff[4][2];
#pragma unroll
  for (int rt = 0; rt < 4; rt++)
#pragma unroll
    for (int ks = 0; ks < 2; ks++) {
      int row = wr * 64 + rt * 16 + r16, chunk = ks * 4 + g;
      aoff[rt][ks] = (row * 8 + (chunk ^ ((row >> 1) & 7))) * 16;
    }
  int boff[7];
#pragma unroll
  for (int tt = 0; tt < 7; tt++) {
    int tau = wc * 7 + tt;
    boff[tt] = G1_A + (28 * (8 * g + q) + 2 * (tau ^ (g & 1)) + (p >> 1)) * 16 + 8 * (p & 1);
  }

  f32x4 acc[4][7];
#pragma unroll
  for (int i = 0; i < 4; i++)
#pragma unroll
    for (int j = 0; j < 7; j++) acc[i][j] = (f32x4){0.f, 0.f, 0.f, 0.f};

  issue(0, 0);
  issue(1, 1);
  constexpr int NKT = D_ / 64;
#pragma unroll 1
  for (int kt = 0; kt < NKT; kt++) {
    if (kt + 1 < NKT) WAIT_V(11); else WAIT_V(0);
    __builtin_amdgcn_s_barrier();
    if (kt + 2 < NKT) issue(kt + 2, (kt + 2) % G1_NST);
    const char* base = smem + (kt % G1_NST) * G1_STAGE;
#pragma unroll
    for (int ks = 0; ks < 2; ks++) {
      bf16x8 af[4], bfr[7];
#pragma unroll
      for (int rt = 0; rt < 4; rt++) af[rt] = *(const bf16x8*)(base + aoff[rt][ks]);
#pragma unroll
      for (int tt = 0; tt < 7; tt++) {
        s16x4 lo = trread(base + boff[tt] + ks * 32 * 448);
        s16x4 hi = trread(base + boff[tt] + ks * 32 * 448 + 4 * 448);
        bfr[tt] = __builtin_shufflevector(lo, hi, 0, 1, 2, 3, 4, 5, 6, 7);
      }
#pragma unroll
      for (int rt = 0; rt < 4; rt++)
#pragma unroll
        for (int tt = 0; tt < 7; tt++)
          acc[rt][tt] = __builtin_amdgcn_mfma_f32_16x16x32_bf16(bfr[tt], af[rt], acc[rt][tt], 0, 0, 0);
    }
  }
  const int half = lane >> 5;
#pragma unroll
  for (int rp = 0; rp < 2; rp++)
#pragma unroll
    for (int tt = 0; tt < 7; tt++) {
      float hv[4];
#pragma unroll
      for (int i = 0; i < 4; i++) {
        unsigned xa = __float_as_uint(acc[2 * rp][tt][i]), ya = __float_as_uint(acc[2 * rp + 1][tt][i]);
        auto r = __builtin_amdgcn_permlane32_swap(xa, ya, false, false);
        float gg = rbf(__uint_as_float(r[0])), uu = rbf(__uint_as_float(r[1]));
        hv[i] = gg * uu;
      }
      int row = m0 + wr * 64 + (2 * rp + half) * 16 + r16;
      int col = n0 + 8 * (wc * 7 + tt) + 4 * (g & 1);
      u32x2 o = {pack2(hv[0], hv[1]), pack2(hv[2], hv[3])};
      *(u32x2*)(h + row * F_ + col) = o;
    }
}

constexpr int CV_U = 33312;
constexpr int CV_LDS = CV_U + 4576;
__device__ __forceinline__ int cv_cb(int b) { return 4160 * b + ((b >> 2) << 5); }
__global__ __launch_bounds__(256) void k_conv(const u16* __restrict__ uT, const u16* __restrict__ Kf,
                                              const u16* __restrict__ Dv, u16* __restrict__ zT) {
  __shared__ __attribute__((aligned(16))) char smem[CV_LDS];
  const int tid = threadIdx.x, lane = tid & 63;
  const int w = __builtin_amdgcn_readfirstlane(tid >> 6);
  const int d = blockIdx.x;
  const u16* Kd = Kf + d * S_;
  {
    const int i = tid;
    u32x4 lo = (i <= 254) ? *(const u32x4*)(Kd + 2032 - 8 * i) : (u32x4){0, 0, 0, 0};
    u32x4 hi = *(const u32x4*)(Kd + 2040 - 8 * i);
    u32x4 uu = *(const u32x4*)(uT + d * S_ + 8 * tid);
    unsigned Wd[8] = {lo[0], lo[1], lo[2], lo[3], hi[0], hi[1], hi[2], hi[3]};
    unsigned Vd[8], Vo[7];
#pragma unroll
    for (int j = 0; j < 8; j++) Vd[j] = __builtin_amdgcn_alignbit(Wd[7 - j], Wd[7 - j], 16);
#pragma unroll
    for (int j = 0; j < 7; j++) Vo[j] = __builtin_amdgcn_alignbit(Vd[j + 1], Vd[j], 16);
#pragma unroll
    for (int b = 0; b < 8; b++) {
      const int sg = 7 - b;
      u32x4 c;
      if (sg & 1) c = (u32x4){Vo[(sg - 1) / 2], Vo[(sg - 1) / 2 + 1], Vo[(sg - 1) / 2 + 2], Vo[(sg - 1) / 2 + 3]};
      else c = (u32x4){Vd[sg / 2], Vd[sg / 2 + 1], Vd[sg / 2 + 2], Vd[sg / 2 + 3]};
      *(u32x4*)(smem + cv_cb(b) + 16 * i) = c;
    }
    if (tid < 8) *(u32x4*)(smem + cv_cb(tid) + 16 * 256) = (u32x4){0, 0, 0, 0};
    *(u32x4*)(smem + CV_U + 480 + 16 * tid) = uu;
    if (tid >= 64 && tid < 94) *(u32x4*)(smem + CV_U + 16 * (tid - 64)) = (u32x4){0, 0, 0, 0};
  }
  __syncthreads();
  const int c = lane & 15, g = lane >> 4;
  const int ja = c >> 3, jb = c & 7;
  f32x4 acc[8];
#pragma unroll
  for (int n = 0; n < 8; n++) acc[n] = (f32x4){0.f, 0.f, 0.f, 0.f};
  const int abase = cv_cb(jb) + 2 * (2040 - 16 - 8 * ja + 8 * g);
  const int bbase = CV_U + 2 * (240 + 16 * c + 8 * g - 16);
#pragma unroll
  for (int mm = 0; mm < 2; mm++) {
    const int m2 = 2 * w + mm;
    bf16x8 A[8], G[8];
#pragma unroll
    for (int p = 0; p < 8; p++) A[p] = *(const bf16x8*)(smem + abase - 64 * m2 - 512 * p);
#pragma unroll
    for (int nu = 0; nu < 8; nu++) G[nu] = *(const bf16x8*)(smem + bbase + 512 * nu - 64 * m2);
#pragma unroll
    for (int n = 0; n < 8; n++)
#pragma unroll
      for (int p = 0; p <= n; p++)
        acc[n] = __builtin_amdgcn_mfma_f32_16x16x32_bf16(A[p], G[n - p], acc[n], 0, 0, 0);
  }
  __syncthreads();
#pragma unroll
  for (int n = 0; n < 8; n++) *(f32x4*)(smem + w * 8192 + n * 1024 + lane * 16) = acc[n];
  __syncthreads();
  const float dd = bflo(Dv[d]);
#pragma unroll
  for (int nn = 0; nn < 2; nn++) {
    const int n = 2 * w + nn;
    f32x4 s = *(const f32x4*)(smem + n * 1024 + lane * 16);
#pragma unroll
    for (int ww = 1; ww < 4; ww++) s += *(const f32x4*)(smem + ww * 8192 + n * 1024 + lane * 16);
    const int t = 256 * n + 16 * c + 4 * g;
    u32x2 uu = *(const u32x2*)(smem + CV_U + 480 + 2 * t);
    float z0 = rbf(s[0]) + rbf(bflo(uu[0]) * dd);
    float z1 = rbf(s[1]) + rbf(bfhi(uu[0]) * dd);
    float z2 = rbf(s[2]) + rbf(bflo(uu[1]) * dd);
    float z3 = rbf(s[3]) + rbf(bfhi(uu[1]) * dd);
    u32x2 o = {pack2(z0, z1), pack2(z2, z3)};
    *(u32x2*)(zT + d * S_ + t) = o;
  }
}

constexpr int G2_A = 8192, G2_B = 11264, G2_STAGE = G2_A + G2_B;
constexpr int G2_LDS = 4 * 2 * G2_STAGE;
__global__ __launch_bounds__(256) void k_gemm2(const u16* __restrict__ h, const u16* __restrict__ wd,
                                               const u16* __restrict__ x, const u16* __restrict__ xn,
                                               const u16* __restrict__ zT, u16* __restrict__ out) {
  extern __shared__ __attribute__((aligned(16))) char smem[];
  const int tid = threadIdx.x, lane = tid & 63;
  const int w = __builtin_amdgcn_readfirstlane(tid >> 6);
  const int bx = blockIdx.x, xcd = bx & 7, idx = bx >> 3;
  const int mt = (xcd & 3) * 8 + (idx & 7), nt = (xcd >> 2) * 4 + (idx >> 3);
  const int m0 = mt * 64, n0 = nt * 80;
  char* wbase = smem + w * 2 * G2_STAGE;

  unsigned asrc[8], bsrc[11];
#pragma unroll
  for (int i = 0; i < 8; i++) {
    int slot = i * 64 + lane, row = slot >> 3, chunk = (slot & 7) ^ ((row >> 1) & 7);
    asrc[i] = ((m0 + row) * F_ + chunk * 8 + w * 64) * 2;
  }
#pragma unroll
  for (int i = 0; i < 11; i++) {
    int slot = i * 64 + lane, grp = slot / 88, rem = slot % 88;
    if (rem >= 80) rem = 0;
    int kk = 8 * grp + rem / 10, piece = rem % 10;
    bsrc[i] = ((kk + w * 64) * D_ + n0 + piece * 8) * 2;
  }
  const unsigned sm0 = lds_off(wbase);
  auto issue = [&](int it, int buf) {
    unsigned base = sm0 + buf * G2_STAGE;
#pragma unroll
    for (int i = 0; i < 8; i++) glds16(asrc[i], h + it * 256, base + i * 1024);
#pragma unroll
    for (int i = 0; i < 11; i++) glds16(bsrc[i], wd + it * 256 * D_, base + G2_A + i * 1024);
  };
  const int g = lane >> 4, r16 = lane & 15, q = r16 >> 2, p = lane & 3;
  int aoff[4][2];
#pragma unroll
  for (int rt = 0; rt < 4; rt++)
#pragma unroll
    for (int ks = 0; ks < 2; ks++) {
      int row = rt * 16 + r16, chunk = ks * 4 + g;
      aoff[rt][ks] = (row * 8 + (chunk ^ ((row >> 1) & 7))) * 16;
    }
  const int boff = G2_A + (88 * g + 10 * q + (p >> 1)) * 16 + 8 * (p & 1);

  f32x4 acc[4][5];
#pragma unroll
  for (int i = 0; i < 4; i++)
#pragma unroll
    for (int j = 0; j < 5; j++) acc[i][j] = (f32x4){0.f, 0.f, 0.f, 0.f};

  const int nit = (w < 3) ? 7 : 6;
  issue(0, 0);
  issue(1, 1);
#pragma unroll 1
  for (int it = 0; it < nit; it++) {
    if (it + 1 < nit) WAIT_V(19); else WAIT_V(0);
    const char* base = wbase + (it & 1) * G2_STAGE;
#pragma unroll
    for (int ks = 0; ks < 2; ks++) {
      bf16x8 af[4], bfr[5];
#pragma unroll
      for (int rt = 0; rt < 4; rt++) af[rt] = *(const bf16x8*)(base + aoff[rt][ks]);
#pragma unroll
      for (int tt = 0; tt < 5; tt++) {
        s16x4 lo = trread(base + boff + ks * 4 * 88 * 16 + tt * 32);
        s16x4 hi = trread(base + boff + ks * 4 * 88 * 16 + tt * 32 + 40 * 16);
        bfr[tt] = __builtin_shufflevector(lo, hi, 0, 1, 2, 3, 4, 5, 6, 7);
      }
#pragma unroll
      for (int rt = 0; rt < 4; rt++)
#pragma unroll
        for (int tt = 0; tt < 5; tt++)
          acc[rt][tt] = __builtin_amdgcn_mfma_f32_16x16x32_bf16(bfr[tt], af[rt], acc[rt][tt], 0, 0, 0);
    }
    WAIT_L0();
    if (it + 2 < nit) issue(it + 2, it & 1);
  }
  __syncthreads();
  float* red = (float*)smem;
#pragma unroll
  for (int rt = 0; rt < 4; rt++)
#pragma unroll
    for (int tt = 0; tt < 5; tt++)
      *(f32x4*)(red + (w * 64 + rt * 16 + r16) * 84 + tt * 16 + 4 * g) = acc[rt][tt];
  __syncthreads();
#pragma unroll
  for (int j = 0; j < 5; j++) {
    int item = tid + 256 * j;
    int t = item / 20, dq = item % 20;
    f32x4 s = *(const f32x4*)(red + t * 84 + dq * 4);
#pragma unroll
    for (int ww = 1; ww < 4; ww++) s += *(const f32x4*)(red + (ww * 64 + t) * 84 + dq * 4);
    const int gt = m0 + t, gd = n0 + dq * 4;
    u32x2 xv = *(const u32x2*)(x + gt * D_ + gd);
    u32x2 nv = *(const u32x2*)(xn + gt * D_ + gd);
    float z[4];
#pragma unroll
    for (int i = 0; i < 4; i++) z[i] = bflo(zT[(gd + i) * S_ + gt]);
    float o0 = rbf(bflo(xv[0]) + rbf(bflo(nv[0]) * z[0])) + rbf(s[0]);
    float o1 = rbf(bfhi(xv[0]) + rbf(bfhi(nv[0]) * z[1])) + rbf(s[1]);
    float o2 = rbf(bflo(xv[1]) + rbf(bflo(nv[1]) * z[2])) + rbf(s[2]);
    float o3 = rbf(bfhi(xv[1]) + rbf(bfhi(nv[1]) * z[3])) + rbf(s[3]);
    u32x2 o = {pack2(o0, o1), pack2(o2, o3)};
    *(u32x2*)(out + gt * D_ + gd) = o;
  }
}

extern "C" void kernel_launch(void* const* d_in, const int* in_sizes, int n_in, void* d_out, int out_size,
                              void* d_ws, size_t ws_size, hipStream_t stream) {
  const u16* x = (const u16*)d_in[0];
  const u16* k = (const u16*)d_in[1];
  const u16* v = (const u16*)d_in[2];
  const u16* sw = (const u16*)d_in[3];
  const u16* Kf = (const u16*)d_in[4];
  const u16* Dv = (const u16*)d_in[5];
  const u16* wg = (const u16*)d_in[7];
  const u16* wu = (const u16*)d_in[8];
  const u16* wd = (const u16*)d_in[9];
  u16* out = (u16*)d_out;
  char* ws = (char*)d_ws;
  u16* xn = (u16*)(ws);
  u16* uT = (u16*)(ws + (4 << 20));
  u16* zT = (u16*)(ws + (8 << 20));
  u16* h = (u16*)(ws + (12 << 20));
  hipFuncSetAttribute((const void*)k_gemm1, hipFuncAttributeMaxDynamicSharedMemorySize, G1_NST * G1_STAGE);
  hipFuncSetAttribute((const void*)k_gemm2, hipFuncAttributeMaxDynamicSharedMemorySize, G2_LDS);
#define ONLY 15
  if (ONLY & 1) k_prep<<<256 + 320, 256, 0, stream>>>(x, k, v, sw, xn, uT);
  if (ONLY & 2) k_gemm1<<<256, 256, G1_NST * G1_STAGE, stream>>>(xn, wg, wu, h);
  if (ONLY & 4) k_conv<<<640, 256, 0, stream>>>(uT, Kf, Dv, zT);
  if (ONLY & 8) k_gemm2<<<256, 256, G2_LDS, stream>>>(h, wd, x, xn, zT, out);
}
```

```cpp
#include <hip/hip_runtime.h>
#include <hip/hip_bf16.h>
#include <stdint.h>

typedef unsigned short u16;
typedef __attribute__((ext_vector_type(8))) short bf16x8;
typedef __attribute__((ext_vector_type(4))) short s16x4;
typedef __attribute__((ext_vector_type(4))) float f32x4;
typedef __attribute__((ext_vector_type(2))) float f32x2;
typedef __attribute__((ext_vector_type(4))) unsigned u32x4;
typedef __attribute__((ext_vector_type(2))) unsigned u32x2;
typedef __attribute__((ext_vector_type(2))) __bf16 bf16x2_t;
#define LDS_AS __attribute__((address_space(3)))
#define GLB_AS __attribute__((address_space(1)))

#define ABL 0
constexpr int S_ = 2048, D_ = 640, F_ = 1728;

__device__ __forceinline__ float bflo(unsigned v) { return __uint_as_float(v << 16); }
__device__ __forceinline__ float bfhi(unsigned v) { return __uint_as_float(v & 0xffff0000u); }
__device__ __forceinline__ unsigned pack2(float a, float b) {
  f32x2 v = {a, b};
  bf16x2_t r = __builtin_convertvector(v, bf16x2_t);
  return __builtin_bit_cast(unsigned, r);
}
__device__ __forceinline__ float rbf(float a) { __bf16 r = (__bf16)a; return (float)r; }

__device__ __forceinline__ void glds16(unsigned voff, const void* sbase, unsigned lds_addr) {
  asm volatile("s_mov_b32 m0, %2\n\ts_nop 0\n\tglobal_load_lds_dwordx4 %0, %1" ::"v"(voff), "s"(sbase), "s"(lds_addr) : "memory");
}
__device__ __forceinline__ void glds16v(const void* vptr, unsigned lds_addr) {
  asm volatile("s_mov_b32 m0, %1\n\ts_nop 0\n\tglobal_load_lds_dwordx4 %0, off" ::"v"(vptr), "s"(lds_addr) : "memory");
}
__device__ __forceinline__ u32x4 gload16(const void* p) {
  u32x4 r;
  asm volatile("global_load_dwordx4 %0, %1, off" : "=v"(r) : "v"(p) : "memory");
  return r;
}
__device__ __forceinline__ unsigned lds_off(const void* p) { return (unsigned)(uintptr_t)(const LDS_AS char*)p; }
__device__ __forceinline__ s16x4 trread(const char* lds) {
  return __builtin_amdgcn_ds_read_tr16_b64_v4i16((LDS_AS s16x4*)lds);
}
#define WAIT_V(n) asm volatile("s_waitcnt vmcnt(" #n ")" ::: "memory")
#define WAIT_L0() asm volatile("s_waitcnt lgkmcnt(0)" ::: "memory")

__device__ __forceinline__ float wave_sum(float v) {
#pragma unroll
  for (int o = 32; o >= 1; o >>= 1) v += __shfl_xor(v, o, 64);
  return v;
}

#define STAMPS 0
#define SPIN_MAX 2000
#define STAMP_DECL
#define STAMP1(i)
#define STAMPR1(i)
#define STAMP_OUT1
#define STAMP2(i)
#define STAMPR2(i)
#define STAMP_OUT2
constexpr int G1_A = 16384, G1_B = 28672, G1_STAGE = G1_A + G1_B;
constexpr int G1_TR = G1_STAGE;
constexpr int G1_SC = 0;
constexpr int G1_CS = 24576;
constexpr int G1_GU = G1_TR + 32768;
constexpr int G1_LDS = G1_GU + 128 * 464;
#define SGB(m, n) __builtin_amdgcn_sched_group_barrier(m, n, 0)
#define BAR() asm volatile("s_barrier" ::: "memory")
template <int N> __device__ __forceinline__ void waitv() { asm volatile("s_waitcnt vmcnt(%0)" ::"n"(N) : "memory"); }

__global__ __launch_bounds__(512) void k1(const u16* __restrict__ x, const u16* __restrict__ kin,
                                          const u16* __restrict__ vin,
                                          const u16* __restrict__ wg, const u16* __restrict__ wu,
                                          const u16* __restrict__ wd, u16* __restrict__ h,
                                          u16* __restrict__ trT, u16* __restrict__ wdp,
                                          float* __restrict__ stats, u16* __restrict__ sT) {
  extern __shared__ __attribute__((aligned(16))) char smem[];
  const int tid = threadIdx.x, lane = tid & 63;
  const int w8 = __builtin_amdgcn_readfirstlane(tid >> 6);
  const int bx = blockIdx.x, xcd = bx & 7, idx = bx >> 3;
  const int mp = (xcd & 1) * 8 + (idx & 7), nw = (xcd >> 1) * 4 + (idx >> 3);
  const int m0 = mp * 128, n0 = (108 * nw) & ~7;
  const int odd = nw & 1;
  constexpr int NKT = D_ / 64;
  STAMP_DECL
  STAMP1(0); STAMPR1(30);
  if (w8 >= 4) {
    const int w = w8 - 4;
    unsigned asrc[4];
    const u16* bsrc[7];
#pragma unroll
    for (int i = 0; i < 4; i++) {
      int slot = (w * 4 + i) * 64 + lane, row = slot >> 3, chunk = (slot & 7) ^ ((row >> 1) & 7);
      asrc[i] = ((m0 + row) * D_ + chunk * 8) * 2;
    }
#pragma unroll
    for (int i = 0; i < 7; i++) {
      int slot = (w * 7 + i) * 64 + lane, kk = slot / 28, p = slot % 28;
      int mat = p & 1, tau = (p >> 1) ^ ((kk >> 3) & 1);
      bsrc[i] = (mat ? wu : wg) + (kk * F_ + n0 + 8 * tau);
    }
    const unsigned sm0 = lds_off(smem);
    auto issue = [&](int kt, int buf) {
      unsigned base = sm0 + buf * G1_STAGE;
#pragma unroll
      for (int i = 0; i < 4; i++) glds16(asrc[i], x + kt * 64, base + (w * 4 + i) * 1024);
#pragma unroll
      for (int i = 0; i < 7; i++) glds16v(bsrc[i] + kt * 64 * F_, base + G1_A + (w * 7 + i) * 1024);
    };
    issue(0, 0);
    issue(1, 1);
    waitv<11>();
    BAR();
    issue(2, 2);
    int buf = 0;
#pragma unroll 1
    for (int kt = 0; kt < NKT - 3; kt++) {
      waitv<11>();
      BAR();
      issue(kt + 3, buf);
      buf = (buf == 2) ? 0 : buf + 1;
    }
    waitv<11>();
    BAR();
    if (bx < 240) {
      const unsigned tb = sm0 + G1_TR + w * 8192;
      int tdst[2];
#pragma unroll
      for (int z = 0; z < 2; z++) {
        const int job = 2 * bx + z, arr = job / 160, tile = job % 160;
        const int t0 = (tile / 10) * 128 + w * 32, d0 = (tile % 10) * 64;
        const u16* src = arr == 0 ? kin : (arr == 1 ? vin : x);
        const unsigned so = ((t0 + (lane >> 3)) * D_ + d0 + 8 * (lane & 7)) * 2;
#pragma unroll
        for (int i = 0; i < 4; i++) glds16(so, src + i * 8 * D_, tb + z * 4096 + i * 1024);
        tdst[z] = (arr * D_ + d0 + (lane & 15)) * S_ + t0 + 8 * (lane >> 4);
      }
      waitv<8>();
      BAR();
      BAR();
      STAMP1(21);
      BAR();
      STAMP1(22);
      waitv<0>();
      STAMP1(23);
      const char* tl = smem + G1_TR + w * 8192 + (8 * (lane >> 4) + ((lane & 15) >> 2)) * 128 + (lane & 3) * 8;
#pragma unroll
      for (int z = 0; z < 2; z++)
#pragma unroll
        for (int c = 0; c < 4; c++) {
          s16x4 lo = trread(tl + z * 4096 + c * 32);
          s16x4 hi = trread(tl + z * 4096 + c * 32 + 512);
          bf16x8 o = __builtin_shufflevector(lo, hi, 0, 1, 2, 3, 4, 5, 6, 7);
          *(bf16x8*)(trT + tdst[z] + 16 * c * S_) = o;
        }
    } else {
      waitv<0>();
      BAR();
      BAR();
      BAR();
    }
  } else {
    const int w = w8;
    {
      const u32x4 sent = {0xffffffffu, 0xffffffffu, 0xffffffffu, 0xffffffffu};
      u32x4* sp = (u32x4*)sT + bx * 640 + tid;
      if (bx >= 40) {
        const int k0 = (bx - 40) * 8;
        u32x4 sr[3];
#pragma unroll
        for (int j = 0; j < 3; j++) { int s = tid + 256 * j; s = s < 640 ? s : 0; sr[j] = *(const u32x4*)(wd + k0 * D_ + s * 8); }
        sp[0] = sent; sp[256] = sent;
        if (tid < 128) sp[512] = sent;
#pragma unroll
        for (int j = 0; j < 3; j++) {
          const int s = tid + 256 * j;
          if (s < 640) {
            const int r = s / 80, c = s % 80, nt = c / 10, piece = c % 10, k = k0 + r;
            const int pos = (k & ~15) | ((k & 4) << 1) | ((k & 8) >> 1) | (k & 3);
            *(u32x4*)(wdp + ((nt * F_ + pos) * 10 + piece) * 8) = sr[j];
          }
        }
      } else {
        sp[0] = sent; sp[256] = sent;
        if (tid < 128) sp[512] = sent;
      }
    }
    const int wr = w >> 1, wc = w & 1;
    const int g = lane >> 4, r16 = lane & 15, q = r16 >> 2, p = lane & 3;
    int aptr[2];
#pragma unroll
    for (int ks = 0; ks < 2; ks++) {
      int row = wr * 64 + r16, chunk = ks * 4 + g;
      aptr[ks] = (row * 8 + (chunk ^ ((row >> 1) & 7))) * 16;
    }
    int taus[7];
#pragma unroll
    for (int tt = 0; tt < 7; tt++) {
      int j = (tt == 0) ? 0 : 1 + ((tt - 1 + 3 * wr) % 6);
      taus[tt] = wc ? 13 - j : j;
    }
    int bptr[7];
#pragma unroll
    for (int tt = 0; tt < 7; tt++)
      bptr[tt] = G1_A + (28 * (8 * g + q) + 2 * (taus[tt] ^ (g & 1)) + (p >> 1)) * 16 + 8 * (p & 1);
    const bool patch = (wc != odd) && (r16 == (odd ? 0 : 7));
    const int arot = wc * 4096;

    f32x4 acc[4][7], accg[2], accc[4];
#pragma unroll
    for (int i = 0; i < 4; i++)
#pragma unroll
      for (int j = 0; j < 7; j++) acc[i][j] = (f32x4){0.f, 0.f, 0.f, 0.f};
#pragma unroll
    for (int i = 0; i < 2; i++) accg[i] = (f32x4){0.f, 0.f, 0.f, 0.f};
#pragma unroll
    for (int i = 0; i < 4; i++) accc[i] = (f32x4){0.f, 0.f, 0.f, 0.f};
    const bf16x8 ones = {0x3F80, 0x3F80, 0x3F80, 0x3F80, 0x3F80, 0x3F80, 0x3F80, 0x3F80};
    int ap0[2] = {aptr[0] + arot, aptr[1] + arot};
    int ap1[2] = {aptr[0] + (arot ^ 4096), aptr[1] + (arot ^ 4096)};

    bf16x8 af0[4], bf0[7], af1[4], bf1[7];
    auto rd = [&](int ks, bf16x8* af, bf16x8* bfr) {
#pragma unroll
      for (int tt = 0; tt < 7; tt++) {
        s16x4 lo = trread(smem + bptr[tt] + ks * 32 * 448);
        s16x4 hi = trread(smem + bptr[tt] + ks * 32 * 448 + 4 * 448);
        bfr[tt] = __builtin_shufflevector(lo, hi, 0, 1, 2, 3, 4, 5, 6, 7);
      }
      af[0] = *(const bf16x8*)(smem + ap0[ks]);
      af[1] = *(const bf16x8*)(smem + ap0[ks] + 2048);
      af[2] = *(const bf16x8*)(smem + ap1[ks]);
      af[3] = *(const bf16x8*)(smem + ap1[ks] + 2048);
    };
    auto mm = [&](bf16x8* af, bf16x8* bfr) {
      bf16x8 b0 = patch ? ones : bfr[0];
#pragma unroll
      for (int rt = 0; rt < 4; rt++)
#pragma unroll
        for (int tt = 1; tt < 7; tt++)
          acc[rt][tt] = __builtin_amdgcn_mfma_f32_16x16x32_bf16(bfr[tt], af[rt], acc[rt][tt], 0, 0, 0);
#pragma unroll
      for (int rt = 0; rt < 4; rt++)
        acc[rt][0] = __builtin_amdgcn_mfma_f32_16x16x32_bf16(b0, af[rt], acc[rt][0], 0, 0, 0);
#pragma unroll
      for (int j = 0; j < 2; j++) accg[j] = __builtin_amdgcn_mfma_f32_16x16x32_bf16(af[j], af[j], accg[j], 0, 0, 0);
      accc[0] = __builtin_amdgcn_mfma_f32_16x16x32_bf16(b0, ones, accc[0], 0, 0, 0);
#pragma unroll
      for (int j = 1; j < 4; j++) accc[j] = __builtin_amdgcn_mfma_f32_16x16x32_bf16(bfr[j], ones, accc[j], 0, 0, 0);
    };
    auto sched = [&]() {
#pragma unroll
      for (int i = 0; i < 9; i++) { SGB(0x8, 1); SGB(0x100, 1); SGB(0x8, 1); SGB(0x100, 1); SGB(0x8, 1); SGB(0x2, 1); }
      SGB(0x8, 7);
    };
    STAMP1(1);
    BAR();
    STAMP1(2);
    rd(0, af0, bf0);
    int buf = 0;
#pragma unroll 1
    for (int kt = 0; kt < NKT; kt++) {
      const int delta = (buf == 2) ? -2 * G1_STAGE : G1_STAGE;
      buf = (buf == 2) ? 0 : buf + 1;
      __builtin_amdgcn_sched_barrier(0);
      rd(1, af1, bf1);
      mm(af0, bf0);
      sched();
      __builtin_amdgcn_sched_barrier(0);
      WAIT_L0();
      BAR();
      __builtin_amdgcn_sched_barrier(0);
#pragma unroll
      for (int tt = 0; tt < 7; tt++) bptr[tt] += delta;
      ap0[0] += delta; ap0[1] += delta; ap1[0] += delta; ap1[1] += delta;
      rd(0, af0, bf0);
      mm(af1, bf1);
      sched();
      __builtin_amdgcn_sched_barrier(0);
    }
    STAMP1(21);
    {
      char* sc = smem + G1_SC + w * 6144 + lane * 16;
      *(f32x4*)(sc) = accg[0];
      *(f32x4*)(sc + 1024) = accg[1];
#pragma unroll
      for (int rt = 0; rt < 4; rt++) *(f32x4*)(sc + 2048 + rt * 1024) = acc[rt][0];
      if (r16 == 0) {
#pragma unroll
        for (int j = 0; j < 4; j++) *(f32x4*)(smem + G1_CS + taus[j] * 64 + 16 * g) = accc[j];
      }
    }
    WAIT_L0();
    BAR();
    STAMP1(22);
    float rs[4], rq[4];
    f32x4 cs[7];
    {
      const int we = odd ? 0 : 1;
#pragma unroll
      for (int rt = 0; rt < 4; rt++) {
        const int T = (rt + 2 * wc) & 3;
        rq[rt] = *(const float*)(smem + G1_SC + (wr * 2 + (T >> 1)) * 6144 + (T & 1) * 1024 + (r16 + 16 * q) * 16 + 4 * (r16 & 3));
        rs[rt] = *(const float*)(smem + G1_SC + (wr * 2 + we) * 6144 + 2048 + ((T + 2 * we) & 3) * 1024 + (r16 + 16 * we) * 16 + 12 * we);
      }
#pragma unroll
      for (int tt = 0; tt < 7; tt++) cs[tt] = *(const f32x4*)(smem + G1_CS + taus[tt] * 64 + 16 * g);
    }
    float mi[4], ir[4], mm_[4], sd_[4];
#pragma unroll
    for (int rt = 0; rt < 4; rt++) {
      const float mean = rs[rt] * (1.0f / D_);
      const float var = fmaxf(rq[rt] * (1.0f / D_) - mean * mean, 0.f);
      mm_[rt] = rbf(mean);
      sd_[rt] = rbf(__builtin_amdgcn_sqrtf(rbf(rbf(var) + 1e-5f)));
      ir[rt] = __builtin_amdgcn_rcpf(sd_[rt]);
      mi[rt] = -mm_[rt] * ir[rt];
    }
    char* gu0 = smem + G1_GU + (wr * 64 + wc * 32 + r16) * 464 + 8 * g;
    char* gu1 = smem + G1_GU + (wr * 64 + (wc ^ 1) * 32 + r16) * 464 + 8 * g;
#pragma unroll
    for (int tt = 0; tt < 7; tt++) {
#pragma unroll
      for (int rt = 0; rt < 4; rt++) {
        f32x4 v = acc[rt][tt] * ir[rt] + cs[tt] * mi[rt];
        u32x2 o = {pack2(v[0], v[1]), pack2(v[2], v[3])};
        *(u32x2*)(((rt & 2) ? gu1 : gu0) + (rt & 1) * 16 * 464 + taus[tt] * 32) = o;
      }
    }
    if (nw == 0 && wc == 0 && g == 0) {
#pragma unroll
      for (int rt = 0; rt < 4; rt++) *(f32x2*)(stats + 2 * (m0 + wr * 64 + rt * 16 + r16)) = (f32x2){mm_[rt], sd_[rt]};
    }
  }
  const int prow = tid / 14, pc = tid % 14;
  const char* gsrc = smem + G1_GU + prow * 464 + pc * 32;
  u16* dst = h + (m0 + prow) * F_ + n0 + pc * 8;
  const bool edge = pc == (odd ? 0 : 13);
  WAIT_L0();
  STAMP1(24);
  BAR();
  STAMP1(25);
  if (tid < 448) {
    u32x4 gv[4], uv[4];
#pragma unroll
    for (int j = 0; j < 4; j++) {
      gv[j] = *(const u32x4*)(gsrc + j * 32 * 464);
      uv[j] = *(const u32x4*)(gsrc + j * 32 * 464 + 16);
    }
#pragma unroll
    for (int j = 0; j < 4; j++) {
      u32x4 o;
#pragma unroll
      for (int i = 0; i < 4; i++) o[i] = pack2(bflo(gv[j][i]) * bflo(uv[j][i]), bfhi(gv[j][i]) * bfhi(uv[j][i]));
      u16* dd = dst + j * 32 * F_;
      if (edge) {
        if (odd) *(u32x2*)(dd + 4) = (u32x2){o[2], o[3]};
        else *(u32x2*)dd = (u32x2){o[0], o[1]};
      } else {
        *(u32x4*)dd = o;
      }
    }
  }
  STAMP1(26);
  STAMP1(27); STAMPR1(31);
  STAMP_OUT1;
}

constexpr int CV_U = 65536;
constexpr int G2_A = 8192, G2_STAGE = 18432, G2_NKT = F_ / 64;
constexpr int G2_SS = 3 * G2_STAGE;
constexpr int G2_BS = 70656;
constexpr int G2_FL = 80896;
constexpr int K2_LDS = 81920;
__device__ __forceinline__ int cv_cb(int b) { return 4160 * b + ((b >> 2) << 5); }

struct CvIn { u32x4 klo, khi; u32x2 kp, kc, vv, xx; };
__device__ __forceinline__ void cv_load(CvIn& in, const int d, const int tid, const int t0, const u16* __restrict__ Kf,
                                        const u16* __restrict__ trT) {
  const u16* Kd = Kf + d * S_;
  const int i = tid & 255;
  in.klo = *(const u32x4*)(Kd + (i <= 254 ? 2032 - 8 * i : 0));
  in.khi = *(const u32x4*)(Kd + 2040 - 8 * i);
  const u16* kd = trT + d * S_ + t0;
  in.kp = *(const u32x2*)(kd - (t0 > 0 ? 4 : 0));
  in.kc = *(const u32x2*)(kd);
  in.vv = *(const u32x2*)(kd + D_ * S_);
  in.xx = *(const u32x2*)(kd + 2 * D_ * S_);
}
__device__ __forceinline__ void cv_task(const CvIn& in, char* smem, const int d, const int tid, const int lane, const int w,
                                        const int t0, const u16* __restrict__ sw, const u16* __restrict__ Dv,
                                        const f32x4 st01, const f32x4 st23, u16* __restrict__ sT) {
  const u32x2 wv = *(const u32x2*)(sw + d * 4);
  const float w0 = bflo(wv[0]), w1 = bfhi(wv[0]), w2 = bflo(wv[1]), w3 = bfhi(wv[1]);
  const float dd = bflo(Dv[d]);
  float kvf[4];
  u32x2 kvp;
  {
    const unsigned kp0 = t0 > 0 ? in.kp[0] : 0u, kp1 = t0 > 0 ? in.kp[1] : 0u;
    const float kk[7] = {bfhi(kp0), bflo(kp1), bfhi(kp1), bflo(in.kc[0]), bfhi(in.kc[0]), bflo(in.kc[1]), bfhi(in.kc[1])};
    const float vf[4] = {bflo(in.vv[0]), bfhi(in.vv[0]), bflo(in.vv[1]), bfhi(in.vv[1])};
    float r[4];
#pragma unroll
    for (int e = 0; e < 4; e++) {
      float a = rbf(kk[e] * w3);
      a = rbf(a + rbf(kk[e + 1] * w2));
      a = rbf(a + rbf(kk[e + 2] * w1));
      a = rbf(a + rbf(kk[e + 3] * w0));
      r[e] = a * vf[e];
    }
    kvp = (u32x2){pack2(r[0], r[1]), pack2(r[2], r[3])};
    kvf[0] = bflo(kvp[0]); kvf[1] = bfhi(kvp[0]); kvf[2] = bflo(kvp[1]); kvf[3] = bfhi(kvp[1]);
  }
  BAR();
  *(u32x2*)(smem + CV_U + 480 + 2 * t0) = kvp;
  if (tid < 256) {
    const int i = tid;
    const u32x4 lo = (i <= 254) ? in.klo : (u32x4){0, 0, 0, 0};
    const unsigned Wd[8] = {lo[0], lo[1], lo[2], lo[3], in.khi[0], in.khi[1], in.khi[2], in.khi[3]};
    unsigned Vd[8], Vo[7];
#pragma unroll
    for (int j = 0; j < 8; j++) Vd[j] = __builtin_amdgcn_alignbit(Wd[7 - j], Wd[7 - j], 16);
#pragma unroll
    for (int j = 0; j < 7; j++) Vo[j] = __builtin_amdgcn_alignbit(Vd[j + 1], Vd[j], 16);
#pragma unroll
    for (int b = 0; b < 8; b++) {
      const int sg = 7 - b;
      u32x4 c;
      if (sg & 1) c = (u32x4){Vo[(sg - 1) / 2], Vo[(sg - 1) / 2 + 1], Vo[(sg - 1) / 2 + 2], Vo[(sg - 1) / 2 + 3]};
      else c = (u32x4){Vd[sg / 2], Vd[sg / 2 + 1], Vd[sg / 2 + 2], Vd[sg / 2 + 3]};
      *(u32x4*)(smem + cv_cb(b) + 16 * i) = c;
    }
  } else if (tid < 264) {
    *(u32x4*)(smem + cv_cb(tid - 256) + 16 * 256) = (u32x4){0, 0, 0, 0};
  } else if (tid >= 320 && tid < 350) {
    *(u32x4*)(smem + CV_U + 16 * (tid - 320)) = (u32x4){0, 0, 0, 0};
  }
  WAIT_L0();
  BAR();
  const int c = lane & 15, g = lane >> 4;
  f32x4 acc[8];
#pragma unroll
  for (int n = 0; n < 8; n++) acc[n] = (f32x4){0.f, 0.f, 0.f, 0.f};
  {
    const char* ab = smem + cv_cb(c & 7) + 2 * (2040 - 16 - 8 * (c >> 3) + 8 * g) - 64 * w;
    const char* bb = smem + CV_U + 2 * (240 + 16 * c + 8 * g - 16) - 64 * w;
    bf16x8 G[8];
#pragma unroll
    for (int nu = 0; nu < 8; nu++) G[nu] = *(const bf16x8*)(bb + 512 * nu);
#pragma unroll
    for (int p = 0; p < 8; p++) {
      const bf16x8 A = *(const bf16x8*)(ab - 512 * p);
#pragma unroll
      for (int n = p; n < 8; n++) acc[n] = __builtin_amdgcn_mfma_f32_16x16x32_bf16(A, G[n - p], acc[n], 0, 0, 0);
    }
  }
  WAIT_L0();
  BAR();
#pragma unroll
  for (int n = 0; n < 8; n++) *(f32x4*)(smem + w * 8192 + n * 1024 + lane * 16) = acc[n];
  WAIT_L0();
  BAR();
  f32x4 s = *(const f32x4*)(smem + w * 1024 + lane * 16);
#pragma unroll
  for (int ww = 1; ww < 8; ww++) s += *(const f32x4*)(smem + ww * 8192 + w * 1024 + lane * 16);
  const float xf[4] = {bflo(in.xx[0]), bfhi(in.xx[0]), bflo(in.xx[1]), bfhi(in.xx[1])};
  const float mf[4] = {st01[0], st01[2], st23[0], st23[2]};
  const float sf[4] = {st01[1], st01[3], st23[1], st23[3]};
  float o[4];
#pragma unroll
  for (int e = 0; e < 4; e++) {
    const float z = rbf(rbf(s[e]) + rbf(kvf[e] * dd));
    const float a = rbf(xf[e] - mf[e]);
    const float r = __builtin_amdgcn_rcpf(sf[e]);
    float qq = a * r;
    qq = fmaf(fmaf(-qq, sf[e], a), r, qq);
    o[e] = xf[e] + rbf(rbf(qq) * z);
  }
  typedef __attribute__((ext_vector_type(2))) unsigned short u16x2;
  const u16x2 cap = {0xfffe, 0xfffe};
  u32x2 ov;
  ov[0] = __builtin_bit_cast(unsigned, __builtin_elementwise_min(__builtin_bit_cast(u16x2, pack2(o[0], o[1])), cap));
  ov[1] = __builtin_bit_cast(unsigned, __builtin_elementwise_min(__builtin_bit_cast(u16x2, pack2(o[2], o[3])), cap));
  asm volatile("global_store_dwordx2 %0, %1, off sc1" ::"v"(sT + d * S_ + t0), "v"(ov) : "memory");
  WAIT_L0();
}

__device__ __forceinline__ u32x4 gload16_sc1(const void* p) {
  u32x4 r;
  asm volatile("global_load_dwordx4 %0, %1, off sc1" : "=v"(r) : "v"(p) : "memory");
  return r;
}
typedef __attribute__((ext_vector_type(2))) unsigned short u16x2;
__device__ __forceinline__ unsigned pkmax(unsigned a, unsigned b) {
  return __builtin_bit_cast(unsigned, __builtin_elementwise_max(__builtin_bit_cast(u16x2, a), __builtin_bit_cast(u16x2, b)));
}
template <int NB>
__device__ __forceinline__ void g2_stage_s(char* smem, const u32x4* sv, const int w, const int pb, const int lane) {
  unsigned m = 0;
#pragma unroll
  for (int j = 0; j < NB; j++) {
    m = pkmax(m, pkmax(pkmax(sv[j][0], sv[j][1]), pkmax(sv[j][2], sv[j][3])));
    *(u32x4*)(smem + G2_SS + ((pb + j) * 8 + (lane >> 3)) * 144 + (lane & 7) * 16) = sv[j];
  }
  const bool bad = ((m & 0xffffu) == 0xffffu) || ((m >> 16) == 0xffffu);
  const unsigned long long bl = __builtin_amdgcn_ballot_w64(bad);
  if (lane == 0) *(unsigned*)(smem + G2_FL + 4 * w) = bl ? 1u : 0u;
}

template <int NB>
__device__ __forceinline__ void g2_producer(char* smem, const int w, const int lane, const int m0, const int nt,
                                            const u16* __restrict__ h, const u16* __restrict__ wdp,
                                            const u16* __restrict__ sT) {
  constexpr int NP = 2 + NB;
  const int pb = (NB == 3) ? 3 * w : 2 * w + 2;
  unsigned asrc[2];
#pragma unroll
  for (int i = 0; i < 2; i++) {
    int row = (2 * w + i) * 8 + (lane >> 3), chunk = (lane & 7) ^ ((row >> 1) & 7);
    asrc[i] = ((m0 + row) * F_ + chunk * 8) * 2;
  }
  const unsigned bsrc = (unsigned)(nt * F_ * 160 + pb * 1024 + lane * 16);
  const unsigned sm0 = lds_off(smem);
  auto issue = [&](int kt) {
    const unsigned base = sm0 + (kt & 3) * G2_STAGE;
#pragma unroll
    for (int i = 0; i < 2; i++) glds16(asrc[i], h + kt * 64, base + (2 * w + i) * 1024);
#pragma unroll
    for (int i = 0; i < NB; i++) glds16(bsrc, wdp + kt * 5120 + i * 512, base + G2_A + (pb + i) * 1024);
  };
  issue(0); issue(1); issue(2); issue(3);
  waitv<3 * NP>();
  BAR();
#pragma unroll 1
  for (int kt = 0; kt < G2_NKT - 4; kt++) {
    waitv<2 * NP>();
    BAR();
    issue(kt + 4);
  }
  u32x4 sv[NB];
  const u16* sp = sT + (nt * 80 + pb * 8 + (lane >> 3)) * S_ + m0 + (lane & 7) * 8;
#pragma unroll
  for (int j = 0; j < NB; j++) sv[j] = gload16_sc1(sp + j * 8 * S_);
  waitv<2 * NP + NB>();
  BAR();
  waitv<NP + NB>();
  BAR();
  waitv<NB>();
  BAR();
  BAR();
  if (NB == 3) asm volatile("s_waitcnt vmcnt(0)" : "+v"(sv[0]), "+v"(sv[1]), "+v"(sv[NB - 1])::"memory");
  else asm volatile("s_waitcnt vmcnt(0)" : "+v"(sv[0]), "+v"(sv[1])::"memory");
  g2_stage_s<NB>(smem, sv, w, pb, lane);
}
template <int NB>
__device__ __forceinline__ void g2_reload_s(char* smem, const int w, const int lane, const int m0, const int nt,
                                            const u16* __restrict__ sT) {
  const int pb = (NB == 3) ? 3 * w : 2 * w + 2;
  u32x4 sv[NB];
  const u16* sp = sT + (nt * 80 + pb * 8 + (lane >> 3)) * S_ + m0 + (lane & 7) * 8;
#pragma unroll
  for (int j = 0; j < NB; j++) sv[j] = gload16_sc1(sp + j * 8 * S_);
  if (NB == 3) asm volatile("s_waitcnt vmcnt(0)" : "+v"(sv[0]), "+v"(sv[1]), "+v"(sv[NB - 1])::"memory");
  else asm volatile("s_waitcnt vmcnt(0)" : "+v"(sv[0]), "+v"(sv[1])::"memory");
  g2_stage_s<NB>(smem, sv, w, pb, lane);
}

__global__ __launch_bounds__(512, 4) void k2(const u16* __restrict__ h, const u16* __restrict__ wdp,
                                             const u16* __restrict__ trT, const u16* __restrict__ Kf,
                                             const u16* __restrict__ sw, const u16* __restrict__ Dv,
                                             const float* __restrict__ stats, u16* __restrict__ sT,
                                             u16* __restrict__ out) {
  extern __shared__ __attribute__((aligned(16))) char smem[];
  const int tid = threadIdx.x, lane = tid & 63;
  const int w8 = __builtin_amdgcn_readfirstlane(tid >> 6);
  const int bx = blockIdx.x;
  const int t0 = 256 * w8 + 16 * (lane & 15) + 4 * (lane >> 4);
  STAMP_DECL
  STAMP2(0); STAMPR2(30);
  if (bx >= 256) {
    const int i = bx - 256;
    CvIn in0, in1, in2;
    cv_load(in0, i, tid, t0, Kf, trT);
    const f32x4 st01 = *(const f32x4*)(stats + 2 * t0), st23 = *(const f32x4*)(stats + 2 * t0 + 4);
    cv_load(in1, i + 256, tid, t0, Kf, trT);
    cv_load(in2, (i < 128) ? i + 512 : i, tid, t0, Kf, trT);
    STAMP2(1);
    cv_task(in0, smem, i, tid, lane, w8, t0, sw, Dv, st01, st23, sT);
    STAMP2(2);
    cv_task(in1, smem, i + 256, tid, lane, w8, t0, sw, Dv, st01, st23, sT);
    STAMP2(3);
    if (i < 128) cv_task(in2, smem, i + 512, tid, lane, w8, t0, sw, Dv, st01, st23, sT);
    STAMP2(4);
    STAMP2(5); STAMPR2(31);
    STAMP_OUT2;
    return;
  }
  const int xcd = bx & 7, idx = bx >> 3;
  const int mt = (xcd & 3) * 8 + (idx & 7), nt = (xcd >> 2) * 4 + (idx >> 3);
  const int m0 = mt * 64, n0 = nt * 80;
  if (w8 >= 4) {
    if (w8 < 6) g2_producer<3>(smem, w8 - 4, lane, m0, nt, h, wdp, sT);
    else g2_producer<2>(smem, w8 - 4, lane, m0, nt, h, wdp, sT);
  } else {
    const int wr = w8 & 1, kh = w8 >> 1;
    const int g = lane >> 4, r16 = lane & 15, q = r16 >> 2, p = lane & 3;
    int aoff[2];
#pragma unroll
    for (int rt = 0; rt < 2; rt++) {
      int row = wr * 32 + rt * 16 + r16, chunk = kh * 4 + g;
      aoff[rt] = (row * 8 + (chunk ^ ((row >> 1) & 7))) * 16;
    }
    const int boff = G2_A + (32 * kh + 16 * (g >> 1) + 4 * (g & 1) + q) * 160 + p * 8;
    f32x4 acc[2][5];
#pragma unroll
    for (int i = 0; i < 2; i++)
#pragma unroll
      for (int j = 0; j < 5; j++) acc[i][j] = (f32x4){0.f, 0.f, 0.f, 0.f};
    bf16x8 af0[2], bf0[5], af1[2], bf1[5];
    auto rd = [&](int kt, bf16x8* af, bf16x8* bfr) {
      const char* base = smem + (kt & 3) * G2_STAGE;
#pragma unroll
      for (int rt = 0; rt < 2; rt++) af[rt] = *(const bf16x8*)(base + aoff[rt]);
#pragma unroll
      for (int tt = 0; tt < 5; tt++) {
        s16x4 lo = trread(base + boff + tt * 32);
        s16x4 hi = trread(base + boff + tt * 32 + 1280);
        bfr[tt] = __builtin_shufflevector(lo, hi, 0, 1, 2, 3, 4, 5, 6, 7);
      }
    };
    auto mm = [&](bf16x8* af, bf16x8* bfr) {
#pragma unroll
      for (int tt = 0; tt < 5; tt++)
#pragma unroll
        for (int rt = 0; rt < 2; rt++)
          acc[rt][tt] = __builtin_amdgcn_mfma_f32_16x16x32_bf16(bfr[tt], af[rt], acc[rt][tt], 0, 0, 0);
    };
    auto sched = [&]() {
#pragma unroll
      for (int i = 0; i < 6; i++) { SGB(0x100, 2); SGB(0x8, 1); }
      SGB(0x8, 4);
    };
    BAR();
    STAMP2(1);
    rd(0, af0, bf0);
    WAIT_L0();
#pragma unroll 1
    for (int kt = 0; kt < G2_NKT - 1; kt += 2) {
      BAR();
      __builtin_amdgcn_sched_barrier(0);
      rd(kt + 1, af1, bf1);
      mm(af0, bf0);
      sched();
      __builtin_amdgcn_sched_barrier(0);
      WAIT_L0();
      BAR();
      __builtin_amdgcn_sched_barrier(0);
      rd(kt + 2, af0, bf0);
      mm(af1, bf1);
      sched();
      __builtin_amdgcn_sched_barrier(0);
      WAIT_L0();
    }
    BAR();
    STAMP2(2);
    mm(af0, bf0);
    float* red = (float*)smem;
#pragma unroll
    for (int rt = 0; rt < 2; rt++)
#pragma unroll
      for (int tt = 0; tt < 5; tt++)
        *(f32x4*)(red + (kh * 64 + wr * 32 + rt * 16 + r16) * 84 + tt * 16 + 4 * g) = acc[rt][tt];
  }
  WAIT_L0();
  STAMP2(3);
  BAR();
  STAMP2(4);
  const int i16 = lane & 15, g = lane >> 4;
  u32x4 fl = *(const u32x4*)(smem + G2_FL);
  bool slow = (fl[0] | fl[1] | fl[2] | fl[3]) != 0;
  slow = __builtin_amdgcn_readfirstlane(slow);
  if (!slow) {
#pragma unroll
    for (int r = 0; r < 2; r++) {
      if (r == 1 && w8 >= 2) break;
      const int a = r ? (2 * w8 + (g >> 1)) : (w8 & 3), c = r ? 8 + (g & 1) : 4 * (w8 >> 2) + g;
      const int t = 16 * a + i16;
      const char* sp = smem + G2_SS + (8 * c + (i16 >> 2)) * 144 + 32 * a + 8 * (i16 & 3);
      const s16x4 lo = trread(sp), hi = trread(sp + 4 * 144);
      const float* rp = (const float*)smem + t * 84 + 8 * c;
      const f32x4 b0 = *(const f32x4*)rp + *(const f32x4*)(rp + 64 * 84);
      const f32x4 b1 = *(const f32x4*)(rp + 4) + *(const f32x4*)(rp + 64 * 84 + 4);
      const u32x2 sl = __builtin_bit_cast(u32x2, lo), sh = __builtin_bit_cast(u32x2, hi);
      u32x4 o;
      o[0] = pack2(bflo(sl[0]) + rbf(b0[0]), bfhi(sl[0]) + rbf(b0[1]));
      o[1] = pack2(bflo(sl[1]) + rbf(b0[2]), bfhi(sl[1]) + rbf(b0[3]));
      o[2] = pack2(bflo(sh[0]) + rbf(b1[0]), bfhi(sh[0]) + rbf(b1[1]));
      o[3] = pack2(bflo(sh[1]) + rbf(b1[2]), bfhi(sh[1]) + rbf(b1[3]));
      *(u32x4*)(out + (m0 + t) * D_ + n0 + 8 * c) = o;
    }
  } else {
#pragma unroll
    for (int r = 0; r < 2; r++) {
      if (r == 1 && w8 >= 2) break;
      const int a = r ? (2 * w8 + (g >> 1)) : (w8 & 3), c = r ? 8 + (g & 1) : 4 * (w8 >> 2) + g;
      const int t = 16 * a + i16;
      const float* rp = (const float*)smem + t * 84 + 8 * c;
      const f32x4 b0 = *(const f32x4*)rp + *(const f32x4*)(rp + 64 * 84);
      const f32x4 b1 = *(const f32x4*)(rp + 4) + *(const f32x4*)(rp + 64 * 84 + 4);
      *(u32x4*)(smem + G2_BS + t * 160 + 16 * c) =
          (u32x4){pack2(b0[0], b0[1]), pack2(b0[2], b0[3]), pack2(b1[0], b1[1]), pack2(b1[2], b1[3])};
    }
    int spin = 0;
#pragma unroll 1
    while (true) {
      WAIT_L0();
      BAR();
      if (spin >= SPIN_MAX) {
        const f32x4 st01 = *(const f32x4*)(stats + 2 * t0), st23 = *(const f32x4*)(stats + 2 * t0 + 4);
#pragma unroll 1
        for (int dd = 0; dd < 80; dd++) {
          CvIn in;
          cv_load(in, n0 + dd, tid, t0, Kf, trT);
          cv_task(in, smem, n0 + dd, tid, lane, w8, t0, sw, Dv, st01, st23, sT);
        }
        WAIT_V(0);
        BAR();
      } else {
        __builtin_amdgcn_s_sleep(8);
      }
      spin++;
      if (w8 >= 4) {
        if (w8 < 6) g2_reload_s<3>(smem, w8 - 4, lane, m0, nt, sT);
        else g2_reload_s<2>(smem, w8 - 4, lane, m0, nt, sT);
      }
      WAIT_L0();
      BAR();
      fl = *(const u32x4*)(smem + G2_FL);
      bool sl2 = (fl[0] | fl[1] | fl[2] | fl[3]) != 0;
      if (!__builtin_amdgcn_readfirstlane(sl2)) break;
    }
#pragma unroll
    for (int r = 0; r < 2; r++) {
      if (r == 1 && w8 >= 2) break;
      const int a = r ? (2 * w8 + (g >> 1)) : (w8 & 3), c = r ? 8 + (g & 1) : 4 * (w8 >> 2) + g;
      const int t = 16 * a + i16;
      const char* sp = smem + G2_SS + (8 * c + (i16 >> 2)) * 144 + 32 * a + 8 * (i16 & 3);
      const s16x4 lo = trread(sp), hi = trread(sp + 4 * 144);
      const u32x4 b = *(const u32x4*)(smem + G2_BS + t * 160 + 16 * c);
      const u32x2 sl = __builtin_bit_cast(u32x2, lo), sh = __builtin_bit_cast(u32x2, hi);
      u32x4 o;
      o[0] = pack2(bflo(sl[0]) + bflo(b[0]), bfhi(sl[0]) + bfhi(b[0]));
      o[1] = pack2(bflo(sl[1]) + bflo(b[1]), bfhi(sl[1]) + bfhi(b[1]));
      o[2] = pack2(bflo(sh[0]) + bflo(b[2]), bfhi(sh[0]) + bfhi(b[2]));
      o[3] = pack2(bflo(sh[1]) + bflo(b[3]), bfhi(sh[1]) + bfhi(b[3]));
      *(u32x4*)(out + (m0 + t) * D_ + n0 + 8 * c) = o;
    }
    STAMP2(7);
  }
  STAMP2(5);
  STAMP2(6); STAMPR2(31);
  STAMP_OUT2;
}

extern "C" void kernel_launch(void* const* d_in, const int* in_sizes, int n_in, void* d_out, int out_size,
                              void* d_ws, size_t ws_size, hipStream_t stream) {
  const u16* x = (const u16*)d_in[0];
  const u16* k = (const u16*)d_in[1];
  const u16* v = (const u16*)d_in[2];
  const u16* sw = (const u16*)d_in[3];
  const u16* Kf = (const u16*)d_in[4];
  const u16* Dv = (const u16*)d_in[5];
  const u16* wg = (const u16*)d_in[7];
  const u16* wu = (const u16*)d_in[8];
  const u16* wd = (const u16*)d_in[9];
  u16* out = (u16*)d_out;
  char* ws = (char*)d_ws;
  u16* wdp = (u16*)(ws);
  float* stats = (float*)(ws + (3 << 20));
  u16* sT = (u16*)(ws + (4 << 20));
  u16* h = (u16*)(ws + (8 << 20));
  u16* trT = (u16*)(ws + (16 << 20));
  (void)hipFuncSetAttribute((const void*)k1, hipFuncAttributeMaxDynamicSharedMemorySize, G1_LDS);
  (void)hipFuncSetAttribute((const void*)k2, hipFuncAttributeMaxDynamicSharedMemorySize, K2_LDS);
#define ONLY 15
  if (ONLY & 2) k1<<<256, 512, G1_LDS, stream>>>(x, k, v, wg, wu, wd, h, trT, wdp, stats, sT);
  if (ONLY & 8) k2<<<512, 512, K2_LDS, stream>>>(h, wdp, trT, Kf, sw, Dv, stats, sT, out);
}
```

```cpp
#include <hip/hip_runtime.h>
#include <hip/hip_bf16.h>
#include <stdint.h>

typedef unsigned short u16;
typedef __attribute__((ext_vector_type(8))) short bf16x8;
typedef __attribute__((ext_vector_type(4))) short s16x4;
typedef __attribute__((ext_vector_type(4))) float f32x4;
typedef __attribute__((ext_vector_type(2))) float f32x2;
typedef __attribute__((ext_vector_type(4))) unsigned u32x4;
typedef __attribute__((ext_vector_type(2))) unsigned u32x2;
typedef __attribute__((ext_vector_type(2))) __bf16 bf16x2_t;
#define LDS_AS __attribute__((address_space(3)))
#define GLB_AS __attribute__((address_space(1)))

#define ABL 0
constexpr int S_ = 2048, D_ = 640, F_ = 1728;

__device__ __forceinline__ float bflo(unsigned v) { return __uint_as_float(v << 16); }
__device__ __forceinline__ float bfhi(unsigned v) { return __uint_as_float(v & 0xffff0000u); }
__device__ __forceinline__ unsigned pack2(float a, float b) {
  f32x2 v = {a, b};
  bf16x2_t r = __builtin_convertvector(v, bf16x2_t);
  return __builtin_bit_cast(unsigned, r);
}
__device__ __forceinline__ float rbf(float a) { __bf16 r = (__bf16)a; return (float)r; }

__device__ __forceinline__ void glds16(unsigned voff, const void* sbase, unsigned lds_addr) {
  asm volatile("s_mov_b32 m0, %2\n\ts_nop 0\n\tglobal_load_lds_dwordx4 %0, %1" ::"v"(voff), "s"(sbase), "s"(lds_addr) : "memory");
}
__device__ __forceinline__ void glds16v(const void* vptr, unsigned lds_addr) {
  asm volatile("s_mov_b32 m0, %1\n\ts_nop 0\n\tglobal_load_lds_dwordx4 %0, off" ::"v"(vptr), "s"(lds_addr) : "memory");
}
__device__ __forceinline__ u32x4 gload16(const void* p) {
  u32x4 r;
  asm volatile("global_load_dwordx4 %0, %1, off" : "=v"(r) : "v"(p) : "memory");
  return r;
}
__device__ __forceinline__ unsigned lds_off(const void* p) { return (unsigned)(uintptr_t)(const LDS_AS char*)p; }
__device__ __forceinline__ s16x4 trread(const char* lds) {
  return __builtin_amdgcn_ds_read_tr16_b64_v4i16((LDS_AS s16x4*)lds);
}
#define WAIT_V(n) asm volatile("s_waitcnt vmcnt(" #n ")" ::: "memory")
#define WAIT_L0() asm volatile("s_waitcnt lgkmcnt(0)" ::: "memory")

__device__ __forceinline__ float wave_sum(float v) {
#pragma unroll
  for (int o = 32; o >= 1; o >>= 1) v += __shfl_xor(v, o, 64);
  return v;
}

#define STAMPS 0
#define SPIN_MAX 2000
#define STAMP_DECL
#define STAMP1(i)
#define STAMPR1(i)
#define STAMP_OUT1
#define STAMP2(i)
#define STAMPR2(i)
#define STAMP_OUT2
constexpr int G1_A = 16384, G1_B = 28672, G1_STAGE = G1_A + G1_B;
constexpr int G1_TR = G1_STAGE;
constexpr int G1_SC = 0;
constexpr int G1_CS = 24576;
constexpr int G1_GU = G1_TR + 32768;
constexpr int G1_LDS = G1_GU + 128 * 464;
#define SGB(m, n) __builtin_amdgcn_sched_group_barrier(m, n, 0)
#define BAR() asm volatile("s_barrier" ::: "memory")
template <int N> __device__ __forceinline__ void waitv() { asm volatile("s_waitcnt vmcnt(%0)" ::"n"(N) : "memory"); }

__global__ __launch_bounds__(512) void k1(const u16* __restrict__ x, const u16* __restrict__ kin,
                                          const u16* __restrict__ vin,
                                          const u16* __restrict__ wg, const u16* __restrict__ wu,
                                          const u16* __restrict__ wd, u16* __restrict__ h,
                                          u16* __restrict__ trT, u16* __restrict__ wdp,
                                          float* __restrict__ stats, u16* __restrict__ sT) {
  extern __shared__ __attribute__((aligned(16))) char smem[];
  const int tid = threadIdx.x, lane = tid & 63;
  const int w8 = __builtin_amdgcn_readfirstlane(tid >> 6);
  const int bx = blockIdx.x, xcd = bx & 7, idx = bx >> 3;
  const int mp = (xcd & 1) * 8 + (idx & 7), nw = (xcd >> 1) * 4 + (idx >> 3);
  const int m0 = mp * 128, n0 = (108 * nw) & ~7;
  const int odd = nw & 1;
  constexpr int NKT = D_ / 64;
  STAMP_DECL
  STAMP1(0); STAMPR1(30);
  if (w8 >= 4) {
    const int w = w8 - 4;
    unsigned asrc[4];
    const u16* bsrc[7];
#pragma unroll
    for (int i = 0; i < 4; i++) {
      int slot = (w * 4 + i) * 64 + lane, row = slot >> 3, chunk = (slot & 7) ^ ((row >> 1) & 7);
      asrc[i] = ((m0 + row) * D_ + chunk * 8) * 2;
    }
#pragma unroll
    for (int i = 0; i < 7; i++) {
      int slot = (w * 7 + i) * 64 + lane, kk = slot / 28, p = slot % 28;
      int mat = p & 1, tau = (p >> 1) ^ ((kk >> 3) & 1);
      bsrc[i] = (mat ? wu : wg) + (kk * F_ + n0 + 8 * tau);
    }
    const unsigned sm0 = lds_off(smem);
    auto issue = [&](int kt, int buf) {
      unsigned base = sm0 + buf * G1_STAGE;
#pragma unroll
      for (int i = 0; i < 4; i++) glds16(asrc[i], x + kt * 64, base + (w * 4 + i) * 1024);
#pragma unroll
      for (int i = 0; i < 7; i++) glds16v(bsrc[i] + kt * 64 * F_, base + G1_A + (w * 7 + i) * 1024);
    };
    issue(0, 0);
    issue(1, 1);
    waitv<11>();
    BAR();
    issue(2, 2);
    int buf = 0;
#pragma unroll 1
    for (int kt = 0; kt < NKT - 3; kt++) {
      waitv<11>();
      BAR();
      issue(kt + 3, buf);
      buf = (buf == 2) ? 0 : buf + 1;
    }
    waitv<11>();
    BAR();
    if (bx < 160) {
      const unsigned tb = sm0 + G1_TR + w * 8192;
      int tdst[2];
#pragma unroll
      for (int z = 0; z < 2; z++) {
        const int job = 2 * bx + z, arr = job / 160, tile = job % 160;
        const int t0 = (tile / 10) * 128 + w * 32, d0 = (tile % 10) * 64;
        const u16* src = arr == 0 ? kin : vin;
        const unsigned so = ((t0 + (lane >> 3)) * D_ + d0 + 8 * (lane & 7)) * 2;
#pragma unroll
        for (int i = 0; i < 4; i++) glds16(so, src + i * 8 * D_, tb + z * 4096 + i * 1024);
        tdst[z] = (arr * D_ + d0 + (lane & 15)) * S_ + t0 + 8 * (lane >> 4);
      }
      waitv<8>();
      BAR();
      BAR();
      STAMP1(21);
      BAR();
      STAMP1(22);
      waitv<0>();
      STAMP1(23);
      const char* tl = smem + G1_TR + w * 8192 + (8 * (lane >> 4) + ((lane & 15) >> 2)) * 128 + (lane & 3) * 8;
#pragma unroll
      for (int z = 0; z < 2; z++)
#pragma unroll
        for (int c = 0; c < 4; c++) {
          s16x4 lo = trread(tl + z * 4096 + c * 32);
          s16x4 hi = trread(tl + z * 4096 + c * 32 + 512);
          bf16x8 o = __builtin_shufflevector(lo, hi, 0, 1, 2, 3, 4, 5, 6, 7);
          *(bf16x8*)(trT + tdst[z] + 16 * c * S_) = o;
        }
    } else {
      waitv<0>();
      BAR();
      BAR();
      BAR();
    }
  } else {
    const int w = w8;
    {
      const u32x4 sent = {0xffffffffu, 0xffffffffu, 0xffffffffu, 0xffffffffu};
      u32x4* sp = (u32x4*)sT + bx * 640 + tid;
      if (bx >= 40) {
        const int k0 = (bx - 40) * 8;
        u32x4 sr[3];
#pragma unroll
        for (int j = 0; j < 3; j++) { int s = tid + 256 * j; s = s < 640 ? s : 0; sr[j] = *(const u32x4*)(wd + k0 * D_ + s * 8); }
        sp[0] = sent; sp[256] = sent;
        if (tid < 128) sp[512] = sent;
#pragma unroll
        for (int j = 0; j < 3; j++) {
          const int s = tid + 256 * j;
          if (s < 640) {
            const int r = s / 80, c = s % 80, nt = c / 10, piece = c % 10, k = k0 + r;
            const int pos = (k & ~15) | ((k & 4) << 1) | ((k & 8) >> 1) | (k & 3);
            *(u32x4*)(wdp + ((nt * F_ + pos) * 10 + piece) * 8) = sr[j];
          }
        }
      } else {
        sp[0] = sent; sp[256] = sent;
        if (tid < 128) sp[512] = sent;
      }
    }
    const int wr = w >> 1, wc = w & 1;
    const int g = lane >> 4, r16 = lane & 15, q = r16 >> 2, p = lane & 3;
    int aptr[2];
#pragma unroll
    for (int ks = 0; ks < 2; ks++) {
      int row = wr * 64 + r16, chunk = ks * 4 + g;
      aptr[ks] = (row * 8 + (chunk ^ ((row >> 1) & 7))) * 16;
    }
    int taus[7];
#pragma unroll
    for (int tt = 0; tt < 7; tt++) {
      int j = (tt == 0) ? 0 : 1 + ((tt - 1 + 3 * wr) % 6);
      taus[tt] = wc ? 13 - j : j;
    }
    int bptr[7];
#pragma unroll
    for (int tt = 0; tt < 7; tt++)
      bptr[tt] = G1_A + (28 * (8 * g + q) + 2 * (taus[tt] ^ (g & 1)) + (p >> 1)) * 16 + 8 * (p & 1);
    const bool patch = (wc != odd) && (r16 == (odd ? 0 : 7));
    const int arot = wc * 4096;

    f32x4 acc[4][7], accg[2], accc[4];
#pragma unroll
    for (int i = 0; i < 4; i++)
#pragma unroll
      for (int j = 0; j < 7; j++) acc[i][j] = (f32x4){0.f, 0.f, 0.f, 0.f};
#pragma unroll
    for (int i = 0; i < 2; i++) accg[i] = (f32x4){0.f, 0.f, 0.f, 0.f};
#pragma unroll
    for (int i = 0; i < 4; i++) accc[i] = (f32x4){0.f, 0.f, 0.f, 0.f};
    const bf16x8 ones = {0x3F80, 0x3F80, 0x3F80, 0x3F80, 0x3F80, 0x3F80, 0x3F80, 0x3F80};
    int ap0[2] = {aptr[0] + arot, aptr[1] + arot};
    int ap1[2] = {aptr[0] + (arot ^ 4096), aptr[1] + (arot ^ 4096)};

    bf16x8 af0[4], bf0[7], af1[4], bf1[7];
    auto rd = [&](int ks, bf16x8* af, bf16x8* bfr) {
#pragma unroll
      for (int tt = 0; tt < 7; tt++) {
        s16x4 lo = trread(smem + bptr[tt] + ks * 32 * 448);
        s16x4 hi = trread(smem + bptr[tt] + ks * 32 * 448 + 4 * 448);
        bfr[tt] = __builtin_shufflevector(lo, hi, 0, 1, 2, 3, 4, 5, 6, 7);
      }
      af[0] = *(const bf16x8*)(smem + ap0[ks]);
      af[1] = *(const bf16x8*)(smem + ap0[ks] + 2048);
      af[2] = *(const bf16x8*)(smem + ap1[ks]);
      af[3] = *(const bf16x8*)(smem + ap1[ks] + 2048);
    };
    auto mm = [&](bf16x8* af, bf16x8* bfr) {
      bf16x8 b0 = patch ? ones : bfr[0];
#pragma unroll
      for (int rt = 0; rt < 4; rt++)
#pragma unroll
        for (int tt = 1; tt < 7; tt++)
          acc[rt][tt] = __builtin_amdgcn_mfma_f32_16x16x32_bf16(bfr[tt], af[rt], acc[rt][tt], 0, 0, 0);
#pragma unroll
      for (int rt = 0; rt < 4; rt++)
        acc[rt][0] = __builtin_amdgcn_mfma_f32_16x16x32_bf16(b0, af[rt], acc[rt][0], 0, 0, 0);
#pragma unroll
      for (int j = 0; j < 2; j++) accg[j] = __builtin_amdgcn_mfma_f32_16x16x32_bf16(af[j], af[j], accg[j], 0, 0, 0);
      accc[0] = __builtin_amdgcn_mfma_f32_16x16x32_bf16(b0, ones, accc[0], 0, 0, 0);
#pragma unroll
      for (int j = 1; j < 4; j++) accc[j] = __builtin_amdgcn_mfma_f32_16x16x32_bf16(bfr[j], ones, accc[j], 0, 0, 0);
    };
    auto sched = [&]() {
#pragma unroll
      for (int i = 0; i < 9; i++) { SGB(0x8, 1); SGB(0x100, 1); SGB(0x8, 1); SGB(0x100, 1); SGB(0x8, 1); SGB(0x2, 1); }
      SGB(0x8, 7);
    };
    STAMP1(1);
    BAR();
    STAMP1(2);
    rd(0, af0, bf0);
    int buf = 0;
#pragma unroll 1
    for (int kt = 0; kt < NKT; kt++) {
      const int delta = (buf == 2) ? -2 * G1_STAGE : G1_STAGE;
      buf = (buf == 2) ? 0 : buf + 1;
      __builtin_amdgcn_sched_barrier(0);
      rd(1, af1, bf1);
      mm(af0, bf0);
      sched();
      __builtin_amdgcn_sched_barrier(0);
      WAIT_L0();
      BAR();
      __builtin_amdgcn_sched_barrier(0);
#pragma unroll
      for (int tt = 0; tt < 7; tt++) bptr[tt] += delta;
      ap0[0] += delta; ap0[1] += delta; ap1[0] += delta; ap1[1] += delta;
      rd(0, af0, bf0);
      mm(af1, bf1);
      sched();
      __builtin_amdgcn_sched_barrier(0);
    }
    STAMP1(21);
    {
      char* sc = smem + G1_SC + w * 6144 + lane * 16;
      *(f32x4*)(sc) = accg[0];
      *(f32x4*)(sc + 1024) = accg[1];
#pragma unroll
      for (int rt = 0; rt < 4; rt++) *(f32x4*)(sc + 2048 + rt * 1024) = acc[rt][0];
      if (r16 == 0) {
#pragma unroll
        for (int j = 0; j < 4; j++) *(f32x4*)(smem + G1_CS + taus[j] * 64 + 16 * g) = accc[j];
      }
    }
    WAIT_L0();
    BAR();
    STAMP1(22);
    float rs[4], rq[4];
    f32x4 cs[7];
    {
      const int we = odd ? 0 : 1;
#pragma unroll
      for (int rt = 0; rt < 4; rt++) {
        const int T = (rt + 2 * wc) & 3;
        rq[rt] = *(const float*)(smem + G1_SC + (wr * 2 + (T >> 1)) * 6144 + (T & 1) * 1024 + (r16 + 16 * q) * 16 + 4 * (r16 & 3));
        rs[rt] = *(const float*)(smem + G1_SC + (wr * 2 + we) * 6144 + 2048 + ((T + 2 * we) & 3) * 1024 + (r16 + 16 * we) * 16 + 12 * we);
      }
#pragma unroll
      for (int tt = 0; tt < 7; tt++) cs[tt] = *(const f32x4*)(smem + G1_CS + taus[tt] * 64 + 16 * g);
    }
    float mi[4], ir[4], mm_[4], sd_[4];
#pragma unroll
    for (int rt = 0; rt < 4; rt++) {
      const float mean = rs[rt] * (1.0f / D_);
      const float var = fmaxf(rq[rt] * (1.0f / D_) - mean * mean, 0.f);
      mm_[rt] = rbf(mean);
      sd_[rt] = rbf(__builtin_amdgcn_sqrtf(rbf(rbf(var) + 1e-5f)));
      ir[rt] = __builtin_amdgcn_rcpf(sd_[rt]);
      mi[rt] = -mm_[rt] * ir[rt];
    }
    char* gu0 = smem + G1_GU + (wr * 64 + wc * 32 + r16) * 464 + 8 * g;
    char* gu1 = smem + G1_GU + (wr * 64 + (wc ^ 1) * 32 + r16) * 464 + 8 * g;
#pragma unroll
    for (int tt = 0; tt < 7; tt++) {
#pragma unroll
      for (int rt = 0; rt < 4; rt++) {
        f32x4 v = acc[rt][tt] * ir[rt] + cs[tt] * mi[rt];
        u32x2 o = {pack2(v[0], v[1]), pack2(v[2], v[3])};
        *(u32x2*)(((rt & 2) ? gu1 : gu0) + (rt & 1) * 16 * 464 + taus[tt] * 32) = o;
      }
    }
    if (nw == 0 && wc == 0 && g == 0) {
#pragma unroll
      for (int rt = 0; rt < 4; rt++) *(f32x2*)(stats + 2 * (m0 + wr * 64 + rt * 16 + r16)) = (f32x2){mm_[rt], sd_[rt]};
    }
  }
  const int prow = tid / 14, pc = tid % 14;
  const char* gsrc = smem + G1_GU + prow * 464 + pc * 32;
  u16* dst = h + (m0 + prow) * F_ + n0 + pc * 8;
  const bool edge = pc == (odd ? 0 : 13);
  WAIT_L0();
  STAMP1(24);
  BAR();
  STAMP1(25);
  if (tid < 448) {
    u32x4 gv[4], uv[4];
#pragma unroll
    for (int j = 0; j < 4; j++) {
      gv[j] = *(const u32x4*)(gsrc + j * 32 * 464);
      uv[j] = *(const u32x4*)(gsrc + j * 32 * 464 + 16);
    }
#pragma unroll
    for (int j = 0; j < 4; j++) {
      u32x4 o;
#pragma unroll
      for (int i = 0; i < 4; i++) o[i] = pack2(bflo(gv[j][i]) * bflo(uv[j][i]), bfhi(gv[j][i]) * bfhi(uv[j][i]));
      u16* dd = dst + j * 32 * F_;
      if (edge) {
        if (odd) *(u32x2*)(dd + 4) = (u32x2){o[2], o[3]};
        else *(u32x2*)dd = (u32x2){o[0], o[1]};
      } else {
        *(u32x4*)dd = o;
      }
    }
  }
  STAMP1(26);
  STAMP1(27); STAMPR1(31);
  STAMP_OUT1;
}

#define PRIO_G 2
#define PRIO_C0 3
#define PRIO_C1 0
constexpr int CV_CS = 4160;
constexpr int CV_UO = 4 * CV_CS;
constexpr int CV_R = CV_UO + 4576;
constexpr int CV_OV = 81920 - 8192;
constexpr int G2_A = 8192, G2_STAGE = 18432, G2_NKT = F_ / 64;
constexpr int G2_SS = 3 * G2_STAGE;
constexpr int G2_BS = 44032;
constexpr int G2_FL = 73600;
constexpr int K2_LDS = 81920;
typedef __attribute__((ext_vector_type(2))) unsigned short u16x2;

struct CvKv { u32x4 kk; u32x2 vv; };
struct CvK { u32x2 lo; u32x4 hi; };
struct CvW { unsigned w01, w23, dpair; };
__device__ __forceinline__ void cv_ldkv(CvKv& in, const int d, const int t0, const u16* __restrict__ trT) {
  const u16* kd = trT + d * S_ + t0;
  in.kk = *(const u32x4*)(kd - (t0 > 0 ? 4 : 0));
  in.vv = *(const u32x2*)(kd + D_ * S_);
}
__device__ __forceinline__ void cv_ldk(CvK& in, const int d, const int i, const u16* __restrict__ Kf) {
  const u16* Kd = Kf + d * S_ + 8 * i;
  in.lo = *(const u32x2*)(Kd - (i > 0 ? 4 : 0));
  in.hi = *(const u32x4*)(Kd);
}
__device__ __forceinline__ void cv_ldw(CvW& w, const int d, const u16* __restrict__ sw, const u16* __restrict__ Dv) {
  w.w01 = ((const unsigned*)sw)[2 * d];
  w.w23 = ((const unsigned*)sw)[2 * d + 1];
  w.dpair = ((const unsigned*)Dv)[d >> 1];
}
__device__ __forceinline__ u32x2 cv_kv(const CvKv& in, const CvW& cw, char* reg, const int d, const int t0) {
  const float w0 = bflo(cw.w01), w1 = bfhi(cw.w01), w2 = bflo(cw.w23), w3 = bfhi(cw.w23);
  const float dd = (d & 1) ? bfhi(cw.dpair) : bflo(cw.dpair);
  const unsigned k0 = t0 > 0 ? in.kk[0] : 0u, k1 = t0 > 0 ? in.kk[1] : 0u;
  const unsigned k2 = t0 > 0 ? in.kk[2] : in.kk[0], k3 = t0 > 0 ? in.kk[3] : in.kk[1];
  const float kk[7] = {bfhi(k0), bflo(k1), bfhi(k1), bflo(k2), bfhi(k2), bflo(k3), bfhi(k3)};
  const float vf[4] = {bflo(in.vv[0]), bfhi(in.vv[0]), bflo(in.vv[1]), bfhi(in.vv[1])};
  unsigned kvr[2];
  u32x2 o;
#pragma unroll
  for (int e = 0; e < 4; e += 2) {
    unsigned a = pack2(kk[e] * w3, kk[e + 1] * w3);
    unsigned b = pack2(kk[e + 1] * w2, kk[e + 2] * w2);
    a = pack2(bflo(a) + bflo(b), bfhi(a) + bfhi(b));
    b = pack2(kk[e + 2] * w1, kk[e + 3] * w1);
    a = pack2(bflo(a) + bflo(b), bfhi(a) + bfhi(b));
    b = pack2(kk[e + 3] * w0, kk[e + 4] * w0);
    a = pack2(bflo(a) + bflo(b), bfhi(a) + bfhi(b));
    const unsigned r = pack2(bfhi(a) * vf[e + 1], bflo(a) * vf[e]);
    kvr[e >> 1] = r;
    o[e >> 1] = pack2(bfhi(r) * dd, bflo(r) * dd);
  }
  *(u32x2*)(reg + CV_UO + 2 * (2044 - t0)) = (u32x2){kvr[1], kvr[0]};
  return o;
}
__device__ __forceinline__ void cv_build(const CvK& in, char* reg, const int i) {
  const unsigned l0 = i > 0 ? in.lo[0] : 0u, l1 = i > 0 ? in.lo[1] : 0u;
  const unsigned v2 = __builtin_amdgcn_alignbit(l1, l0, 16), v3 = __builtin_amdgcn_alignbit(in.hi[0], l1, 16);
  const unsigned v4 = __builtin_amdgcn_alignbit(in.hi[1], in.hi[0], 16), v5 = __builtin_amdgcn_alignbit(in.hi[2], in.hi[1], 16);
  const unsigned v6 = __builtin_amdgcn_alignbit(in.hi[3], in.hi[2], 16);
  char* p = reg + 16 * (i + 2);
  *(u32x4*)(p + 3 * CV_CS) = in.hi;
  *(u32x4*)(p) = (u32x4){v2, v3, v4, v5};
  *(u32x4*)(p + CV_CS) = (u32x4){l1, in.hi[0], in.hi[1], in.hi[2]};
  *(u32x4*)(p + 2 * CV_CS) = (u32x4){v3, v4, v5, v6};
  if (i < 38) {
    const int o = i < 8 ? CV_CS * (i >> 1) + 16 * (i & 1) : CV_UO + 4096 + 16 * (i - 8);
    *(u32x4*)(reg + o) = (u32x4){0, 0, 0, 0};
  }
}
__device__ __forceinline__ bf16x8 cv_ra(const char* p) {
  const s16x4 lo = *(const s16x4*)p, hi = *(const s16x4*)(p + 8);
  return __builtin_shufflevector(lo, hi, 0, 1, 2, 3, 4, 5, 6, 7);
}
#define SB0() __builtin_amdgcn_sched_barrier(0)
template <bool MID>
__device__ __forceinline__ void cv_unit(const char* reg, const int lane, const int q, f32x4* acc) {
  const int c = lane & 15, g = lane >> 4;
#pragma unroll
  for (int n = 0; n < 8; n++) acc[n] = (f32x4){0.f, 0.f, 0.f, 0.f};
  const char* ab = reg + CV_CS * (c & 3) + 128 * q + 16 * g + 8 * (c >> 2) + 8;
  const char* bb = reg + CV_UO + 4064 - 32 * c + 128 * q + 16 * g - 512 * 7;
  bf16x8 G[2][8], A[2][8];
  SB0();
  A[0][0] = cv_ra(ab);
#pragma unroll
  for (int nu = 7; nu >= 0; nu--) G[0][nu] = *(const bf16x8*)(bb + 512 * (7 - nu));
  A[0][1] = cv_ra(ab + 512);
#pragma unroll
  for (int s = 0; s < 2; s++) {
#pragma unroll
    for (int p = 0; p < 8; p++) {
      SB0();
      if (p + 2 < 8) A[s][p + 2] = cv_ra(ab + 64 * s + 512 * (p + 2));
      else if (s == 0) A[1][p - 6] = cv_ra(ab + 64 + 512 * (p - 6));
      if (s == 0 && p > 0) G[1][8 - p] = *(const bf16x8*)(bb + 64 + 512 * (p - 1));
      if (s == 1 && p == 0) G[1][0] = *(const bf16x8*)(bb + 64 + 512 * 7);
      SB0();
#pragma unroll
      for (int n = 7; n >= p; n--)
        acc[n] = __builtin_amdgcn_mfma_f32_16x16x32_bf16(A[s][p], G[s][n - p], acc[n], 0, 0, 0);
    }
    SB0();
    if (MID && s == 0) BAR();
  }
}
__device__ __forceinline__ char* cv_slot(char* reg, char* ov, const int k) { return k < 20 ? reg + 1024 * k : ov + 1024 * (k - 20); }
__device__ __forceinline__ void cv_zst(const f32x4 s, const u32x2 kd, u16* dst) {
  const unsigned y0 = pack2(s[0], s[1]), y1 = pack2(s[2], s[3]);
  const u16x2 cap = {0xfffe, 0xfffe};
  u32x2 ov;
  ov[0] = __builtin_bit_cast(unsigned, __builtin_elementwise_min(__builtin_bit_cast(u16x2, pack2(bflo(y0) + bflo(kd[0]), bfhi(y0) + bfhi(kd[0]))), cap));
  ov[1] = __builtin_bit_cast(unsigned, __builtin_elementwise_min(__builtin_bit_cast(u16x2, pack2(bflo(y1) + bflo(kd[1]), bfhi(y1) + bfhi(kd[1]))), cap));
  asm volatile("global_store_dwordx2 %0, %1, off sc1" ::"v"(dst), "v"(ov) : "memory");
}
template <int Q>
__device__ __forceinline__ void cv_wr6t(char* reg, char* ov, const int lane, const f32x4* acc, f32x4& k0, f32x4& k1) {
#pragma unroll
  for (int n = 0; n < 8; n++) {
    const int o = n >> 1;
    if (o != Q) *(f32x4*)(cv_slot(reg, ov, n * 3 + ((Q - o - 1) & 3)) + lane * 16) = acc[n];
  }
  k0 = acc[2 * Q];
  k1 = acc[2 * Q + 1];
}
__device__ __forceinline__ void cv_wr6(char* reg, char* ov, const int lane, const int q, const f32x4* acc, f32x4& k0, f32x4& k1) {
  if (q == 0) cv_wr6t<0>(reg, ov, lane, acc, k0, k1);
  else if (q == 1) cv_wr6t<1>(reg, ov, lane, acc, k0, k1);
  else if (q == 2) cv_wr6t<2>(reg, ov, lane, acc, k0, k1);
  else cv_wr6t<3>(reg, ov, lane, acc, k0, k1);
}
template <int Q>
__device__ __forceinline__ void cv_wr7t(char* r2, const int lane, const f32x4* acc, f32x4& kc) {
#pragma unroll
  for (int n = 0; n < 8; n++) {
    const int o = n >> 1;
    if (n & 1) *(f32x4*)(r2 + (7 * o + 3 + Q) * 1024 + lane * 16) = acc[n];
    else if (o != Q) *(f32x4*)(r2 + (7 * o + ((Q - o - 1) & 3)) * 1024 + lane * 16) = acc[n];
  }
  kc = acc[2 * Q];
}
__device__ __forceinline__ void cv_wr7(char* r2, const int lane, const int q, const f32x4* acc, f32x4& kc) {
  if (q == 0) cv_wr7t<0>(r2, lane, acc, kc);
  else if (q == 1) cv_wr7t<1>(r2, lane, acc, kc);
  else if (q == 2) cv_wr7t<2>(r2, lane, acc, kc);
  else cv_wr7t<3>(r2, lane, acc, kc);
}
__device__ __forceinline__ void cv_epi2(char* reg, char* ov, const int lane, const int q, f32x4 k0, f32x4 k1, const u32x2 kda,
                                        const u32x2 kdb, u16* dst) {
  f32x4 a[3], b[3];
#pragma unroll
  for (int j = 0; j < 3; j++) {
    a[j] = *(const f32x4*)(cv_slot(reg, ov, 6 * q + j) + lane * 16);
    b[j] = *(const f32x4*)(cv_slot(reg, ov, 6 * q + 3 + j) + lane * 16);
  }
  cv_zst((k0 + a[0]) + (a[1] + a[2]), kda, dst);
  cv_zst((k1 + b[0]) + (b[1] + b[2]), kdb, dst + 256);
}
template <bool THREE>
__device__ __forceinline__ void cv_run(char* smem, const int tid, const int lane, const int w8, const int d0, const int d1,
                                       const int d2, const u16* __restrict__ Kf, const u16* __restrict__ trT,
                                       const u16* __restrict__ sw, const u16* __restrict__ Dv, u16* __restrict__ zT
                                       ) {
  const int q = w8 & 3, grp = w8 >> 2, i = tid & 255;
  const int tl = 16 * (lane & 15) + 4 * (lane >> 4);
  const int ta = 512 * q + tl;
  const int tc = 512 * q + 256 * grp + tl;
  const int dm = grp ? d1 : d0;
  char* rm = smem + grp * CV_R;
  char* r2 = smem + 2 * CV_R;
  char* ov = smem + CV_OV + grp * 4096;
  CvK Km, K2;
  CvKv ia, ib, ic;
  CvW wm, w2;
  cv_ldw(wm, dm, sw, Dv);
  cv_ldk(Km, dm, i, Kf);
  cv_ldkv(ia, dm, ta, trT);
  cv_ldkv(ib, dm, ta + 256, trT);
  if (THREE) {
    cv_ldw(w2, d2, sw, Dv);
    cv_ldkv(ic, d2, tc, trT);
    if (grp) cv_ldk(K2, d2, i, Kf);
  }
  asm volatile("" ::: "memory");
  __builtin_amdgcn_s_setprio(PRIO_C1);
  STAMP2(1);
  cv_build(Km, rm, i);
  const u32x2 kda = cv_kv(ia, wm, rm, dm, ta);
  const u32x2 kdb = cv_kv(ib, wm, rm, dm, ta + 256);
  u32x2 kdc;
  if (THREE) kdc = cv_kv(ic, w2, r2, d2, tc);
  WAIT_L0();
  STAMP2(2);
  BAR();
  f32x4 acc[8], k0, k1;
  if (THREE) {
    if (grp) {
      cv_build(K2, r2, i);
      WAIT_L0();
      cv_unit<true>(rm, lane, q, acc);
      WAIT_L0();
      STAMP2(4);
      BAR();
      cv_wr6(rm, ov, lane, q, acc, k0, k1);
      WAIT_L0();
      STAMP2(5);
      BAR();
      cv_epi2(rm, ov, lane, q, k0, k1, kda, kdb, zT + dm * S_ + ta);
      const char* pp = r2 + (7 * q + 3) * 1024 + lane * 16;
      const f32x4 s = (*(const f32x4*)pp + *(const f32x4*)(pp + 1024)) + (*(const f32x4*)(pp + 2048) + *(const f32x4*)(pp + 3072));
      cv_zst(s, kdc, zT + d2 * S_ + tc);
    } else {
      cv_unit<false>(rm, lane, q, acc);
      WAIT_L0();
      STAMP2(3);
      BAR();
      cv_wr6(rm, ov, lane, q, acc, k0, k1);
      cv_unit<false>(r2, lane, q, acc);
      WAIT_L0();
      STAMP2(4);
      BAR();
      f32x4 kc;
      cv_wr7(r2, lane, q, acc, kc);
      cv_epi2(rm, ov, lane, q, k0, k1, kda, kdb, zT + dm * S_ + ta);
      WAIT_L0();
      STAMP2(5);
      BAR();
      const char* pp = r2 + 7 * q * 1024 + lane * 16;
      const f32x4 s = (kc + *(const f32x4*)pp) + (*(const f32x4*)(pp + 1024) + *(const f32x4*)(pp + 2048));
      cv_zst(s, kdc, zT + d2 * S_ + tc);
    }
  } else {
    cv_unit<false>(rm, lane, q, acc);
    WAIT_L0();
    STAMP2(3);
    BAR();
    cv_wr6(rm, ov, lane, q, acc, k0, k1);
    WAIT_L0();
    STAMP2(5);
    BAR();
    cv_epi2(rm, ov, lane, q, k0, k1, kda, kdb, zT + dm * S_ + ta);
  }
}
#define STVP

__device__ __forceinline__ u32x4 gload16_sc1(const void* p) {
  u32x4 r;
  asm volatile("global_load_dwordx4 %0, %1, off sc1" : "=v"(r) : "v"(p) : "memory");
  return r;
}
__device__ __forceinline__ unsigned pkmax(unsigned a, unsigned b) {
  return __builtin_bit_cast(unsigned, __builtin_elementwise_max(__builtin_bit_cast(u16x2, a), __builtin_bit_cast(u16x2, b)));
}
template <int NB>
__device__ __forceinline__ bool g2_bad(const u32x4* sv) {
  unsigned m = 0;
#pragma unroll
  for (int j = 0; j < NB; j++) m = pkmax(m, pkmax(pkmax(sv[j][0], sv[j][1]), pkmax(sv[j][2], sv[j][3])));
  const bool bad = ((m & 0xffffu) == 0xffffu) || ((m >> 16) == 0xffffu);
  return __builtin_amdgcn_ballot_w64(bad) != 0;
}
template <int NB>
__device__ __forceinline__ void g2_wait0(u32x4* sv) {
  if (NB == 3) asm volatile("s_waitcnt vmcnt(0)" : "+v"(sv[0]), "+v"(sv[1]), "+v"(sv[NB - 1])::"memory");
  else asm volatile("s_waitcnt vmcnt(0)" : "+v"(sv[0]), "+v"(sv[1])::"memory");
}
template <int NB>
__device__ __forceinline__ void g2_finish_z(char* smem, u32x4* sv, const u16* sp, const int w, const int pb, const int lane,
                                            const int spin_max) {
  g2_wait0<NB>(sv);
  bool bad = g2_bad<NB>(sv);
#pragma unroll 1
  for (int spin = 0; bad && spin < spin_max; spin++) {
    __builtin_amdgcn_s_sleep(4);
#pragma unroll
    for (int j = 0; j < NB; j++) sv[j] = gload16_sc1(sp + j * 8 * S_);
    g2_wait0<NB>(sv);
    bad = g2_bad<NB>(sv);
  }
#pragma unroll
  for (int j = 0; j < NB; j++) *(u32x4*)(smem + G2_SS + ((pb + j) * 8 + (lane >> 3)) * 144 + (lane & 7) * 16) = sv[j];
  if (lane == 0) *(unsigned*)(smem + G2_FL + 4 * w) = bad ? 1u : 0u;
}

__device__ __forceinline__ u32x4 ln8(const u32x4 xv, const float m, float sd) {
  asm volatile("" : "+v"(sd));
  const float r = __builtin_amdgcn_rcpf(sd);
  u32x4 o;
#pragma unroll
  for (int i = 0; i < 4; i++) {
    const unsigned a = pack2(bflo(xv[i]) - m, bfhi(xv[i]) - m);
    const float a0 = bflo(a), a1 = bfhi(a);
    float q0 = a0 * r, q1 = a1 * r;
    q0 = fmaf(fmaf(-q0, sd, a0), r, q0);
    q1 = fmaf(fmaf(-q1, sd, a1), r, q1);
    o[i] = pack2(q0, q1);
  }
  return o;
}
__device__ __forceinline__ u32x2 gload8(const void* p) {
  u32x2 r;
  asm volatile("global_load_dwordx2 %0, %1, off" : "=v"(r) : "v"(p) : "memory");
  return r;
}
template <int NB>
__device__ __forceinline__ void g2_producer(char* smem, const int w, const int lane, const int m0, const int nt,
                                            const u16* __restrict__ h, const u16* __restrict__ wdp,
                                            const u16* __restrict__ zT, const u16* xa, const u16* xb, const float* sta,
                                            const float* stb, u32x4& xv0, u32x4& xv1, u32x4& xn0, u32x4& xn1) {
  constexpr int NP = 2 + NB;
  const int pb = (NB == 3) ? 3 * w : 2 * w + 2;
  unsigned asrc[2];
#pragma unroll
  for (int i = 0; i < 2; i++) {
    int row = (2 * w + i) * 8 + (lane >> 3), chunk = (lane & 7) ^ ((row >> 1) & 7);
    asrc[i] = ((m0 + row) * F_ + chunk * 8) * 2;
  }
  const unsigned bsrc = (unsigned)(nt * F_ * 160 + pb * 1024 + lane * 16);
  const unsigned sm0 = lds_off(smem);
  auto issue = [&](int kt) {
    const unsigned base = sm0 + (kt & 3) * G2_STAGE;
#pragma unroll
    for (int i = 0; i < 2; i++) glds16(asrc[i], h + kt * 64, base + (2 * w + i) * 1024);
#pragma unroll
    for (int i = 0; i < NB; i++) glds16(bsrc, wdp + kt * 5120 + i * 512, base + G2_A + (pb + i) * 1024);
  };
  u32x4 xa_ = gload16(xa), xb_ = xa_;
  u32x2 sa = gload8(sta), sb = sa;
  if (NB == 3) { xb_ = gload16(xb); sb = gload8(stb); }
  issue(0); issue(1); issue(2); issue(3);
  if (NB == 3) asm volatile("s_waitcnt vmcnt(%4)" : "+v"(xa_), "+v"(sa), "+v"(xb_), "+v"(sb) : "n"(3 * NP) : "memory");
  else asm volatile("s_waitcnt vmcnt(%2)" : "+v"(xa_), "+v"(sa) : "n"(3 * NP) : "memory");
  BAR();
  xv0 = xa_;
  xn0 = ln8(xa_, __uint_as_float(sa[0]), __uint_as_float(sa[1]));
  if (NB == 3) { xv1 = xb_; xn1 = ln8(xb_, __uint_as_float(sb[0]), __uint_as_float(sb[1])); }
  else { xv1 = xa_; xn1 = xn0; }
  asm volatile("" : "+v"(xn0), "+v"(xn1));
#pragma unroll 1
  for (int kt = 0; kt < G2_NKT - 4; kt++) {
    waitv<2 * NP>();
    BAR();
    issue(kt + 4);
  }
  waitv<2 * NP>();
  BAR();
  waitv<NP>();
  BAR();
  u32x4 sv[NB];
  const u16* sp = zT + (nt * 80 + pb * 8 + (lane >> 3)) * S_ + m0 + (lane & 7) * 8;
#pragma unroll
  for (int j = 0; j < NB; j++) sv[j] = gload16_sc1(sp + j * 8 * S_);
  waitv<NB>();
  BAR();
  BAR();
  g2_finish_z<NB>(smem, sv, sp, w, pb, lane, SPIN_MAX);
}
template <int NB>
__device__ __forceinline__ void g2_reload_z(char* smem, const int w, const int lane, const int m0, const int nt,
                                            const u16* __restrict__ zT) {
  const int pb = (NB == 3) ? 3 * w : 2 * w + 2;
  u32x4 sv[NB];
  const u16* sp = zT + (nt * 80 + pb * 8 + (lane >> 3)) * S_ + m0 + (lane & 7) * 8;
#pragma unroll
  for (int j = 0; j < NB; j++) sv[j] = gload16_sc1(sp + j * 8 * S_);
  g2_finish_z<NB>(smem, sv, sp, w, pb, lane, 0);
}
__device__ __forceinline__ unsigned fin2(const unsigned xv, const unsigned xn, const unsigned z, const unsigned b) {
  const unsigned a = pack2(bflo(xn) * bflo(z), bfhi(xn) * bfhi(z));
  const unsigned s = pack2(bflo(xv) + bflo(a), bfhi(xv) + bfhi(a));
  return pack2(bflo(s) + bflo(b), bfhi(s) + bfhi(b));
}

__global__ __launch_bounds__(512, 4) void k2(const u16* __restrict__ h, const u16* __restrict__ wdp,
                                             const u16* __restrict__ trT, const u16* __restrict__ Kf,
                                             const u16* __restrict__ sw, const u16* __restrict__ Dv,
                                             const float* __restrict__ stats, u16* __restrict__ zT,
                                             const u16* __restrict__ x, u16* __restrict__ out) {
  extern __shared__ __attribute__((aligned(16))) char smem[];
  const int tid = threadIdx.x, lane = tid & 63;
  const int w8 = __builtin_amdgcn_readfirstlane(tid >> 6);
  const int bx = blockIdx.x;
  STAMP_DECL
  STAMP2(0); STAMPR2(30);
  if (bx >= 256) {
    __builtin_amdgcn_s_setprio(PRIO_C0);
    const int i = bx - 256;
    if (i < 128) cv_run<true>(smem, tid, lane, w8, i, i + 256, i + 512, Kf, trT, sw, Dv, zT STVP);
    else cv_run<false>(smem, tid, lane, w8, i, i + 256, 0, Kf, trT, sw, Dv, zT STVP);
    STAMP2(9);
    STAMP2(10); STAMPR2(31);
    STAMP_OUT2;
    return;
  }
  __builtin_amdgcn_s_setprio(PRIO_G);
  const int xcd = bx & 7, idx = bx >> 3;
  const int mt = (xcd & 3) * 8 + (idx & 7), nt = (xcd >> 2) * 4 + (idx >> 3);
  const int m0 = mt * 64, n0 = nt * 80;
  const int i16 = lane & 15, g = lane >> 4;
  const int ea0 = w8 & 3, ec0 = 4 * (w8 >> 2) + g;
  const int ea1 = (2 * w8 + (g >> 1)) & 3, ec1 = 8 + (g & 1);
  const bool two = (w8 == 4) || (w8 == 5);
  u32x4 xv0, xn0, xv1, xn1;
  const int ta = m0 + 16 * ea0 + i16, tb = m0 + 16 * ea1 + i16;
  if (w8 >= 4) {
    const u16* xa = x + ta * D_ + n0 + 8 * ec0;
    const u16* xb = x + tb * D_ + n0 + 8 * ec1;
    if (w8 < 6) g2_producer<3>(smem, w8 - 4, lane, m0, nt, h, wdp, zT, xa, xb, stats + 2 * ta, stats + 2 * tb, xv0, xv1, xn0, xn1);
    else g2_producer<2>(smem, w8 - 4, lane, m0, nt, h, wdp, zT, xa, xb, stats + 2 * ta, stats + 2 * tb, xv0, xv1, xn0, xn1);
  } else {
    xv0 = *(const u32x4*)(x + ta * D_ + n0 + 8 * ec0);
    const f32x2 sa = *(const f32x2*)(stats + 2 * ta);
    xn0 = ln8(xv0, sa[0], sa[1]);
    asm volatile("" : "+v"(xn0));
    xn1 = xn0;
    xv1 = xv0;
    const int wr = w8 & 1, kh = w8 >> 1;
    const int r16 = lane & 15, q = r16 >> 2, p = lane & 3;
    int aoff[2];
#pragma unroll
    for (int rt = 0; rt < 2; rt++) {
      int row = wr * 32 + rt * 16 + r16, chunk = kh * 4 + g;
      aoff[rt] = (row * 8 + (chunk ^ ((row >> 1) & 7))) * 16;
    }
    const int boff = G2_A + (32 * kh + 16 * (g >> 1) + 4 * (g & 1) + q) * 160 + p * 8;
    f32x4 acc[2][5];
#pragma unroll
    for (int i = 0; i < 2; i++)
#pragma unroll
      for (int j = 0; j < 5; j++) acc[i][j] = (f32x4){0.f, 0.f, 0.f, 0.f};
    bf16x8 af0[2], bf0[5], af1[2], bf1[5];
    auto rd = [&](int kt, bf16x8* af, bf16x8* bfr) {
      const char* base = smem + (kt & 3) * G2_STAGE;
#pragma unroll
      for (int rt = 0; rt < 2; rt++) af[rt] = *(const bf16x8*)(base + aoff[rt]);
#pragma unroll
      for (int tt = 0; tt < 5; tt++) {
        s16x4 lo = trread(base + boff + tt * 32);
        s16x4 hi = trread(base + boff + tt * 32 + 1280);
        bfr[tt] = __builtin_shufflevector(lo, hi, 0, 1, 2, 3, 4, 5, 6, 7);
      }
    };
    auto mm = [&](bf16x8* af, bf16x8* bfr) {
#pragma unroll
      for (int tt = 0; tt < 5; tt++)
#pragma unroll
        for (int rt = 0; rt < 2; rt++)
          acc[rt][tt] = __builtin_amdgcn_mfma_f32_16x16x32_bf16(bfr[tt], af[rt], acc[rt][tt], 0, 0, 0);
    };
    auto sched = [&]() {
#pragma unroll
      for (int i = 0; i < 6; i++) { SGB(0x100, 2); SGB(0x8, 1); }
      SGB(0x8, 4);
    };
    BAR();
    STAMP2(1);
    rd(0, af0, bf0);
    WAIT_L0();
#pragma unroll 1
    for (int kt = 0; kt < G2_NKT - 1; kt += 2) {
      BAR();
      __builtin_amdgcn_sched_barrier(0);
      rd(kt + 1, af1, bf1);
      mm(af0, bf0);
      sched();
      __builtin_amdgcn_sched_barrier(0);
      WAIT_L0();
      BAR();
      __builtin_amdgcn_sched_barrier(0);
      rd(kt + 2, af0, bf0);
      mm(af1, bf1);
      sched();
      __builtin_amdgcn_sched_barrier(0);
      WAIT_L0();
    }
    BAR();
    STAMP2(2);
    mm(af0, bf0);
    float* red = (float*)smem;
#pragma unroll
    for (int rt = 0; rt < 2; rt++)
#pragma unroll
      for (int tt = 0; tt < 5; tt++)
        *(f32x4*)(red + (kh * 64 + wr * 32 + rt * 16 + r16) * 84 + tt * 16 + 4 * g) = acc[rt][tt];
  }
  WAIT_L0();
  STAMP2(3);
  BAR();
  STAMP2(4);
  const u32x4 fl = *(const u32x4*)(smem + G2_FL);
  bool slow = (fl[0] | fl[1] | fl[2] | fl[3]) != 0;
  slow = __builtin_amdgcn_readfirstlane(slow);
  u32x4 bq[2];
#pragma unroll
  for (int r = 0; r < 2; r++) {
    if (r == 1 && !two) break;
    const int t = 16 * (r ? ea1 : ea0) + i16, c = r ? ec1 : ec0;
    const float* rp = (const float*)smem + t * 84 + 8 * c;
    const f32x4 b0 = *(const f32x4*)rp + *(const f32x4*)(rp + 64 * 84);
    const f32x4 b1 = *(const f32x4*)(rp + 4) + *(const f32x4*)(rp + 64 * 84 + 4);
    bq[r] = (u32x4){pack2(b0[0], b0[1]), pack2(b0[2], b0[3]), pack2(b1[0], b1[1]), pack2(b1[2], b1[3])};
  }
  if (slow) {
    *(u32x4*)(smem + G2_BS + tid * 16) = bq[0];
    if (two) *(u32x4*)(smem + G2_BS + (tid + 256) * 16) = bq[1];
    WAIT_L0();
    BAR();
#pragma unroll 1
    for (int dd = 0; dd < 80; dd += 2) {
      cv_run<false>(smem, tid, lane, w8, n0 + dd, n0 + dd + 1, 0, Kf, trT, sw, Dv, zT STVP);
      WAIT_L0();
      BAR();
    }
    WAIT_V(0);
    BAR();
    if (w8 >= 4) {
      if (w8 < 6) g2_reload_z<3>(smem, w8 - 4, lane, m0, nt, zT);
      else g2_reload_z<2>(smem, w8 - 4, lane, m0, nt, zT);
    }
    WAIT_L0();
    BAR();
    bq[0] = *(const u32x4*)(smem + G2_BS + tid * 16);
    bq[1] = *(const u32x4*)(smem + G2_BS + (two ? tid + 256 : tid) * 16);
    int l2 = threadIdx.x & 63;
    asm volatile("" : "+v"(l2));
    const int ta2 = m0 + 16 * ea0 + (l2 & 15), tb2 = m0 + 16 * ((2 * w8 + (l2 >> 5)) & 3) + (l2 & 15);
    xv0 = *(const u32x4*)(x + ta2 * D_ + n0 + 8 * (4 * (w8 >> 2) + (l2 >> 4)));
    xv1 = *(const u32x4*)(x + tb2 * D_ + n0 + 8 * (8 + ((l2 >> 4) & 1)));
    xn0 = ln8(xv0, stats[2 * ta2], stats[2 * ta2 + 1]);
    xn1 = ln8(xv1, stats[2 * tb2], stats[2 * tb2 + 1]);
    STAMP2(7);
  }
#pragma unroll
  for (int r = 0; r < 2; r++) {
    if (r == 1 && !two) break;
    const int a = r ? ea1 : ea0, c = r ? ec1 : ec0;
    const int t = 16 * a + i16;
    const char* sp = smem + G2_SS + (8 * c + (i16 >> 2)) * 144 + 32 * a + 8 * (i16 & 3);
    const u32x2 zl = __builtin_bit_cast(u32x2, trread(sp)), zh = __builtin_bit_cast(u32x2, trread(sp + 4 * 144));
    const u32x4 xv = r ? xv1 : xv0, xn = r ? xn1 : xn0;
    u32x4 o;
    o[0] = fin2(xv[0], xn[0], zl[0], bq[r][0]);
    o[1] = fin2(xv[1], xn[1], zl[1], bq[r][1]);
    o[2] = fin2(xv[2], xn[2], zh[0], bq[r][2]);
    o[3] = fin2(xv[3], xn[3], zh[1], bq[r][3]);
    *(u32x4*)(out + (m0 + t) * D_ + n0 + 8 * c) = o;
  }
  STAMP2(5);
  STAMP2(6); STAMPR2(31);
  STAMP_OUT2;
}

extern "C" void kernel_launch(void* const* d_in, const int* in_sizes, int n_in, void* d_out, int out_size,
                              void* d_ws, size_t ws_size, hipStream_t stream) {
  const u16* x = (const u16*)d_in[0];
  const u16* k = (const u16*)d_in[1];
  const u16* v = (const u16*)d_in[2];
  const u16* sw = (const u16*)d_in[3];
  const u16* Kf = (const u16*)d_in[4];
  const u16* Dv = (const u16*)d_in[5];
  const u16* wg = (const u16*)d_in[7];
  const u16* wu = (const u16*)d_in[8];
  const u16* wd = (const u16*)d_in[9];
  u16* out = (u16*)d_out;
  char* ws = (char*)d_ws;
  u16* wdp = (u16*)(ws);
  float* stats = (float*)(ws + (3 << 20));
  u16* zT = (u16*)(ws + (4 << 20));
  u16* h = (u16*)(ws + (8 << 20));
  u16* trT = (u16*)(ws + (16 << 20));
  (void)hipFuncSetAttribute((const void*)k1, hipFuncAttributeMaxDynamicSharedMemorySize, G1_LDS);
  (void)hipFuncSetAttribute((const void*)k2, hipFuncAttributeMaxDynamicSharedMemorySize, K2_LDS);
#define ONLY 15
  if (ONLY & 2) k1<<<256, 512, G1_LDS, stream>>>(x, k, v, wg, wu, wd, h, trT, wdp, stats, zT);
  if (ONLY & 8) k2<<<512, 512, K2_LDS, stream>>>(h, wdp, trT, Kf, sw, Dv, stats, zT, x, out);
}
```

```cpp
#include <hip/hip_runtime.h>
#include <hip/hip_bf16.h>
#include <stdint.h>

typedef unsigned short u16;
typedef __attribute__((ext_vector_type(8))) short bf16x8;
typedef __attribute__((ext_vector_type(4))) short s16x4;
typedef __attribute__((ext_vector_type(4))) float f32x4;
typedef __attribute__((ext_vector_type(2))) float f32x2;
typedef __attribute__((ext_vector_type(4))) unsigned u32x4;
typedef __attribute__((ext_vector_type(2))) unsigned u32x2;
typedef __attribute__((ext_vector_type(2))) __bf16 bf16x2_t;
#define LDS_AS __attribute__((address_space(3)))
#define GLB_AS __attribute__((address_space(1)))

#define ABL 0
constexpr int S_ = 2048, D_ = 640, F_ = 1728;

__device__ __forceinline__ float bflo(unsigned v) { return __uint_as_float(v << 16); }
__device__ __forceinline__ float bfhi(unsigned v) { return __uint_as_float(v & 0xffff0000u); }
__device__ __forceinline__ unsigned pack2(float a, float b) {
  f32x2 v = {a, b};
  bf16x2_t r = __builtin_convertvector(v, bf16x2_t);
  return __builtin_bit_cast(unsigned, r);
}
__device__ __forceinline__ float rbf(float a) { __bf16 r = (__bf16)a; return (float)r; }

__device__ __forceinline__ void glds16(unsigned voff, const void* sbase, unsigned lds_addr) {
  asm volatile("s_mov_b32 m0, %2\n\ts_nop 0\n\tglobal_load_lds_dwordx4 %0, %1" ::"v"(voff), "s"(sbase), "s"(lds_addr) : "memory");
}
__device__ __forceinline__ void glds16v(const void* vptr, unsigned lds_addr) {
  asm volatile("s_mov_b32 m0, %1\n\ts_nop 0\n\tglobal_load_lds_dwordx4 %0, off" ::"v"(vptr), "s"(lds_addr) : "memory");
}
__device__ __forceinline__ u32x4 gload16(const void* p) {
  u32x4 r;
  asm volatile("global_load_dwordx4 %0, %1, off" : "=v"(r) : "v"(p) : "memory");
  return r;
}
__device__ __forceinline__ unsigned lds_off(const void* p) { return (unsigned)(uintptr_t)(const LDS_AS char*)p; }
__device__ __forceinline__ s16x4 trread(const char* lds) {
  return __builtin_amdgcn_ds_read_tr16_b64_v4i16((LDS_AS s16x4*)lds);
}
#define WAIT_V(n) asm volatile("s_waitcnt vmcnt(" #n ")" ::: "memory")
#define WAIT_L0() asm volatile("s_waitcnt lgkmcnt(0)" ::: "memory")

__device__ __forceinline__ float wave_sum(float v) {
#pragma unroll
  for (int o = 32; o >= 1; o >>= 1) v += __shfl_xor(v, o, 64);
  return v;
}

#define STAMPS 0
#define SPIN_MAX 2000
#define STAMP_DECL
#define STAMP1(i)
#define STAMPR1(i)
#define STAMP_OUT1
#define STAMP2(i)
#define STAMPR2(i)
#define STAMP_OUT2
#define T1N 3
#define T2AT 0
constexpr int G1_A = 16384, G1_B = 28672, G1_STAGE = G1_A + G1_B;
constexpr int G1_TR = G1_STAGE;
constexpr int G1_SC = 0;
constexpr int G1_CS = 24576;
constexpr int G1_GU = G1_TR + 32768;
constexpr int G1_DUMP = G1_GU + 128 * 464;
constexpr int G1_PK = G1_DUMP + 256;
constexpr int G1_LDS = G1_PK + 12288;
__device__ __forceinline__ void gtouch(unsigned voff, const void* sbase, unsigned lds_addr) {
  asm volatile("s_mov_b32 m0, %2\n\ts_nop 0\n\tglobal_load_lds_dword %0, %1" ::"v"(voff), "s"(sbase), "s"(lds_addr) : "memory");
}
#define SGB(m, n) __builtin_amdgcn_sched_group_barrier(m, n, 0)
#define BAR() asm volatile("s_barrier" ::: "memory")
template <int N> __device__ __forceinline__ void waitv() { asm volatile("s_waitcnt vmcnt(%0)" ::"n"(N) : "memory"); }

__global__ __launch_bounds__(512) void k1(const u16* __restrict__ x, const u16* __restrict__ kin,
                                          const u16* __restrict__ vin,
                                          const u16* __restrict__ wg, const u16* __restrict__ wu,
                                          const u16* __restrict__ wd, u16* __restrict__ h,
                                          u16* __restrict__ trT, u16* __restrict__ wdp,
                                          float* __restrict__ stats, u16* __restrict__ sT,
                                          const u16* __restrict__ Kf) {
  extern __shared__ __attribute__((aligned(16))) char smem[];
  const int tid = threadIdx.x, lane = tid & 63;
  const int w8 = __builtin_amdgcn_readfirstlane(tid >> 6);
  const int bx = blockIdx.x, xcd = bx & 7, idx = bx >> 3;
  const int mp = (xcd & 1) * 8 + (idx & 7), nw = (xcd >> 1) * 4 + (idx >> 3);
  const int m0 = mp * 128, n0 = (108 * nw) & ~7;
  const int odd = nw & 1;
  constexpr int NKT = D_ / 64;
  STAMP_DECL
  STAMP1(0); STAMPR1(30);
  if (w8 >= 4) {
    const int w = w8 - 4;
    const unsigned sm0 = lds_off(smem);
    const bool tw = (w == 1) || (w == 2);
    const u16* tb = tw ? (w == 1 ? wg : wu) : x;
    unsigned tv2;
    {
      const int sl = tw ? lane / 7 : lane >> 3;
      const int ln = 7 * bx + lane % 7;
      const unsigned b0 = tw ? (ln < 1727 ? ln : 1727) * 128 : (8 * bx + (lane & 7)) * (D_ * 2);
      const unsigned sst = tw ? 64 * F_ * 2 : 128;
      gtouch(b0 + (1 + (sl < T1N - 1 ? sl : T1N - 1)) * sst, tb, sm0 + G1_DUMP);
      tv2 = b0 + (T1N + 1 + (sl < 8 - T1N ? sl : 8 - T1N)) * sst;
    }
    unsigned asrc[4];
    const u16* bsrc[7];
#pragma unroll
    for (int i = 0; i < 4; i++) {
      int slot = (w * 4 + i) * 64 + lane, row = slot >> 3, chunk = (slot & 7) ^ ((row >> 1) & 7);
      asrc[i] = ((m0 + row) * D_ + chunk * 8) * 2;
    }
    {
      const u16* wb = ((lane & 1) ? wu : wg) + n0;
      const int q28 = (lane >= 28) + (lane >= 56);
      int kk = 16 * w + q28, p = lane - 28 * q28;
#pragma unroll
      for (int i = 0; i < 7; i++) {
        const int tau = (p >> 1) ^ ((kk >> 3) & 1);
        bsrc[i] = wb + (unsigned)(kk * F_ + 8 * tau);
        p += 8; kk += 2;
        if (p >= 28) { p -= 28; kk++; }
      }
    }
    auto issue = [&](int kt, int buf) {
      unsigned base = sm0 + buf * G1_STAGE;
#pragma unroll
      for (int i = 0; i < 4; i++) glds16(asrc[i], x + kt * 64, base + (w * 4 + i) * 1024);
#pragma unroll
      for (int i = 0; i < 7; i++) glds16v(bsrc[i] + kt * 64 * F_, base + G1_A + (w * 7 + i) * 1024);
    };
    STAMP1(4);
    issue(0, 0);
    STAMP1(5);
    issue(1, 1);
    STAMP1(6);
    waitv<11>();
    STAMP1(8);
    BAR();
    issue(2, 2);
    gtouch(tv2, tb, sm0 + G1_DUMP);
    waitv<12>();
    STAMP1(9);
    BAR();
    STAMP1(16);
    issue(3, 0);
    waitv<12>();
    STAMP1(10);
    BAR();
    STAMP1(17);
    issue(4, 1);
    const int pt = tid - 256;
    waitv<11>();
    STAMP1(11);
    BAR();
    STAMP1(18);
    issue(5, 2);
    {
      const u32x4 sent = {0xffffffffu, 0xffffffffu, 0xffffffffu, 0xffffffffu};
      u32x4* sp = (u32x4*)sT + bx * 640 + pt;
      sp[0] = sent; sp[256] = sent;
      if (pt < 128) sp[512] = sent;
    }
    asm volatile("" ::: "memory");
    int buf = 0;
#pragma unroll 1
    for (int kt = 3; kt < NKT - 3; kt++) {
      waitv<11>();
      BAR();
      issue(kt + 3, buf);
      buf = (buf == 2) ? 0 : buf + 1;
    }
    const int k0 = (bx >= 40 ? bx - 40 : 0) * 8;
#pragma unroll
    for (int j = 0; j < 3; j++) { int s = pt + 256 * j; s = s < 640 ? s : 0; glds16((k0 * D_ + s * 8) * 2, wd, sm0 + G1_PK + j * 4096 + w * 1024); }
    auto pack = [&]() {
      if (bx >= 40) {
#pragma unroll
        for (int j = 0; j < 3; j++) {
          const int s = pt + 256 * j;
          if (s < 640) {
            const int r = s / 80, c = s % 80, nt = c / 10, piece = c % 10, k = k0 + r;
            const int pos = (k & ~15) | ((k & 4) << 1) | ((k & 8) >> 1) | (k & 3);
            *(u32x4*)(wdp + ((nt * F_ + pos) * 10 + piece) * 8) = *(const u32x4*)(smem + G1_PK + s * 16);
          }
        }
      }
    };
    const unsigned kfo = (bx + 256 * ((w < 2 || bx < 128) ? (w < 3 ? w : 0) : 0)) * (S_ * 2) + (lane & 31) * 128;
    waitv<14>();
    BAR();
    if (bx < 160) {
      const unsigned tb = sm0 + G1_TR + w * 8192;
      int tdst[2];
#pragma unroll
      for (int z = 0; z < 2; z++) {
        const int job = 2 * bx + z, arr = job / 160, tile = job % 160;
        const int t0 = (tile / 10) * 128 + w * 32, d0 = (tile % 10) * 64;
        const u16* src = arr == 0 ? kin : vin;
        const unsigned so = ((t0 + (lane >> 3)) * D_ + d0 + 8 * (lane & 7)) * 2;
#pragma unroll
        for (int i = 0; i < 4; i++) glds16(so, src + i * 8 * D_, tb + z * 4096 + i * 1024);
        tdst[z] = (arr * D_ + d0 + (lane & 15)) * S_ + t0 + 8 * (lane >> 4);
      }
      gtouch(kfo, Kf, sm0 + G1_DUMP);
      waitv<12>();
      BAR();
      waitv<9>();
      pack();
      BAR();
      STAMP1(21);
      BAR();
      STAMP1(22);
      waitv<0>();
      STAMP1(23);
      const char* tl = smem + G1_TR + w * 8192 + (8 * (lane >> 4) + ((lane & 15) >> 2)) * 128 + (lane & 3) * 8;
#pragma unroll
      for (int z = 0; z < 2; z++)
#pragma unroll
        for (int c = 0; c < 4; c++) {
          s16x4 lo = trread(tl + z * 4096 + c * 32);
          s16x4 hi = trread(tl + z * 4096 + c * 32 + 512);
          bf16x8 o = __builtin_shufflevector(lo, hi, 0, 1, 2, 3, 4, 5, 6, 7);
          *(bf16x8*)(trT + tdst[z] + 16 * c * S_) = o;
        }
    } else {
      gtouch(kfo, Kf, sm0 + G1_DUMP);
      waitv<4>();
      BAR();
      waitv<1>();
      pack();
      BAR();
      waitv<0>();
      BAR();
    }
  } else {
    const int w = w8;
    const int wr = w >> 1, wc = w & 1;
    const int g = lane >> 4, r16 = lane & 15, q = r16 >> 2, p = lane & 3;
    int aptr[2];
#pragma unroll
    for (int ks = 0; ks < 2; ks++) {
      int row = wr * 64 + r16, chunk = ks * 4 + g;
      aptr[ks] = (row * 8 + (chunk ^ ((row >> 1) & 7))) * 16;
    }
    int taus[7];
#pragma unroll
    for (int tt = 0; tt < 7; tt++) {
      int j = (tt == 0) ? 0 : 1 + ((tt - 1 + 3 * wr) % 6);
      taus[tt] = wc ? 13 - j : j;
    }
    int bptr[7];
#pragma unroll
    for (int tt = 0; tt < 7; tt++)
      bptr[tt] = G1_A + (28 * (8 * g + q) + 2 * (taus[tt] ^ (g & 1)) + (p >> 1)) * 16 + 8 * (p & 1);
    const bool patch = (wc != odd) && (r16 == (odd ? 0 : 7));
    const int arot = wc * 4096;

    f32x4 acc[4][7], accg[2], accc[4];
#pragma unroll
    for (int i = 0; i < 4; i++)
#pragma unroll
      for (int j = 0; j < 7; j++) acc[i][j] = (f32x4){0.f, 0.f, 0.f, 0.f};
#pragma unroll
    for (int i = 0; i < 2; i++) accg[i] = (f32x4){0.f, 0.f, 0.f, 0.f};
#pragma unroll
    for (int i = 0; i < 4; i++) accc[i] = (f32x4){0.f, 0.f, 0.f, 0.f};
    const bf16x8 ones = {0x3F80, 0x3F80, 0x3F80, 0x3F80, 0x3F80, 0x3F80, 0x3F80, 0x3F80};
    int ap0[2] = {aptr[0] + arot, aptr[1] + arot};
    int ap1[2] = {aptr[0] + (arot ^ 4096), aptr[1] + (arot ^ 4096)};

    bf16x8 af0[4], bf0[7], af1[4], bf1[7];
    auto rd = [&](int ks, bf16x8* af, bf16x8* bfr) {
#pragma unroll
      for (int tt = 0; tt < 7; tt++) {
        s16x4 lo = trread(smem + bptr[tt] + ks * 32 * 448);
        s16x4 hi = trread(smem + bptr[tt] + ks * 32 * 448 + 4 * 448);
        bfr[tt] = __builtin_shufflevector(lo, hi, 0, 1, 2, 3, 4, 5, 6, 7);
      }
      af[0] = *(const bf16x8*)(smem + ap0[ks]);
      af[1] = *(const bf16x8*)(smem + ap0[ks] + 2048);
      af[2] = *(const bf16x8*)(smem + ap1[ks]);
      af[3] = *(const bf16x8*)(smem + ap1[ks] + 2048);
    };
    auto mm = [&](bf16x8* af, bf16x8* bfr) {
      bf16x8 b0 = patch ? ones : bfr[0];
#pragma unroll
      for (int rt = 0; rt < 4; rt++)
#pragma unroll
        for (int tt = 1; tt < 7; tt++)
          acc[rt][tt] = __builtin_amdgcn_mfma_f32_16x16x32_bf16(bfr[tt], af[rt], acc[rt][tt], 0, 0, 0);
#pragma unroll
      for (int rt = 0; rt < 4; rt++)
        acc[rt][0] = __builtin_amdgcn_mfma_f32_16x16x32_bf16(b0, af[rt], acc[rt][0], 0, 0, 0);
#pragma unroll
      for (int j = 0; j < 2; j++) accg[j] = __builtin_amdgcn_mfma_f32_16x16x32_bf16(af[j], af[j], accg[j], 0, 0, 0);
      accc[0] = __builtin_amdgcn_mfma_f32_16x16x32_bf16(b0, ones, accc[0], 0, 0, 0);
#pragma unroll
      for (int j = 1; j < 4; j++) accc[j] = __builtin_amdgcn_mfma_f32_16x16x32_bf16(bfr[j], ones, accc[j], 0, 0, 0);
    };
    auto sched = [&]() {
#pragma unroll
      for (int i = 0; i < 9; i++) { SGB(0x8, 1); SGB(0x100, 1); SGB(0x8, 1); SGB(0x100, 1); SGB(0x8, 1); SGB(0x2, 1); }
      SGB(0x8, 7);
    };
    STAMP1(1);
    BAR();
    STAMP1(2);
    rd(0, af0, bf0);
    int buf = 0;
#pragma unroll 1
    for (int kt = 0; kt < NKT; kt++) {
      const int delta = (buf == 2) ? -2 * G1_STAGE : G1_STAGE;
      buf = (buf == 2) ? 0 : buf + 1;
      __builtin_amdgcn_sched_barrier(0);
      rd(1, af1, bf1);
      mm(af0, bf0);
      sched();
      __builtin_amdgcn_sched_barrier(0);
      WAIT_L0();
      BAR();
      __builtin_amdgcn_sched_barrier(0);
#pragma unroll
      for (int tt = 0; tt < 7; tt++) bptr[tt] += delta;
      ap0[0] += delta; ap0[1] += delta; ap1[0] += delta; ap1[1] += delta;
      rd(0, af0, bf0);
      mm(af1, bf1);
      sched();
      __builtin_amdgcn_sched_barrier(0);
    }
    STAMP1(21);
    {
      char* sc = smem + G1_SC + w * 6144 + lane * 16;
      *(f32x4*)(sc) = accg[0];
      *(f32x4*)(sc + 1024) = accg[1];
#pragma unroll
      for (int rt = 0; rt < 4; rt++) *(f32x4*)(sc + 2048 + rt * 1024) = acc[rt][0];
      if (r16 == 0) {
#pragma unroll
        for (int j = 0; j < 4; j++) *(f32x4*)(smem + G1_CS + taus[j] * 64 + 16 * g) = accc[j];
      }
    }
    WAIT_L0();
    BAR();
    STAMP1(22);
    float rs[4], rq[4];
    f32x4 cs[7];
    {
      const int we = odd ? 0 : 1;
#pragma unroll
      for (int rt = 0; rt < 4; rt++) {
        const int T = (rt + 2 * wc) & 3;
        rq[rt] = *(const float*)(smem + G1_SC + (wr * 2 + (T >> 1)) * 6144 + (T & 1) * 1024 + (r16 + 16 * q) * 16 + 4 * (r16 & 3));
        rs[rt] = *(const float*)(smem + G1_SC + (wr * 2 + we) * 6144 + 2048 + ((T + 2 * we) & 3) * 1024 + (r16 + 16 * we) * 16 + 12 * we);
      }
#pragma unroll
      for (int tt = 0; tt < 7; tt++) cs[tt] = *(const f32x4*)(smem + G1_CS + taus[tt] * 64 + 16 * g);
    }
    float mi[4], ir[4], mm_[4], sd_[4];
#pragma unroll
    for (int rt = 0; rt < 4; rt++) {
      const float mean = rs[rt] * (1.0f / D_);
      const float var = fmaxf(rq[rt] * (1.0f / D_) - mean * mean, 0.f);
      mm_[rt] = rbf(mean);
      sd_[rt] = rbf(__builtin_amdgcn_sqrtf(rbf(rbf(var) + 1e-5f)));
      ir[rt] = __builtin_amdgcn_rcpf(sd_[rt]);
      mi[rt] = -mm_[rt] * ir[rt];
    }
    char* gu0 = smem + G1_GU + (wr * 64 + wc * 32 + r16) * 464 + 8 * g;
    char* gu1 = smem + G1_GU + (wr * 64 + (wc ^ 1) * 32 + r16) * 464 + 8 * g;
#pragma unroll
    for (int tt = 0; tt < 7; tt++) {
#pragma unroll
      for (int rt = 0; rt < 4; rt++) {
        f32x4 v = acc[rt][tt] * ir[rt] + cs[tt] * mi[rt];
        u32x2 o = {pack2(v[0], v[1]), pack2(v[2], v[3])};
        *(u32x2*)(((rt & 2) ? gu1 : gu0) + (rt & 1) * 16 * 464 + taus[tt] * 32) = o;
      }
    }
    if (nw == 0 && wc == 0 && g == 0) {
#pragma unroll
      for (int rt = 0; rt < 4; rt++) *(f32x2*)(stats + 2 * (m0 + wr * 64 + rt * 16 + r16)) = (f32x2){mm_[rt], sd_[rt]};
    }
  }
  const int prow = tid / 14, pc = tid % 14;
  const char* gsrc = smem + G1_GU + prow * 464 + pc * 32;
  u16* dst = h + (m0 + prow) * F_ + n0 + pc * 8;
  const bool edge = pc == (odd ? 0 : 13);
  WAIT_L0();
  STAMP1(24);
  BAR();
  STAMP1(25);
  if (tid < 448) {
    u32x4 gv[4], uv[4];
#pragma unroll
    for (int j = 0; j < 4; j++) {
      gv[j] = *(const u32x4*)(gsrc + j * 32 * 464);
      uv[j] = *(const u32x4*)(gsrc + j * 32 * 464 + 16);
    }
#pragma unroll
    for (int j = 0; j < 4; j++) {
      u32x4 o;
#pragma unroll
      for (int i = 0; i < 4; i++) o[i] = pack2(bflo(gv[j][i]) * bflo(uv[j][i]), bfhi(gv[j][i]) * bfhi(uv[j][i]));
      u16* dd = dst + j * 32 * F_;
      if (edge) {
        if (odd) *(u32x2*)(dd + 4) = (u32x2){o[2], o[3]};
        else *(u32x2*)dd = (u32x2){o[0], o[1]};
      } else {
        *(u32x4*)dd = o;
      }
    }
  }
  STAMP1(26);
  STAMP1(27); STAMPR1(31);
  STAMP_OUT1;
}

#define PRIO_G 2
#define PRIO_C0 3
#define PRIO_C1 0
constexpr int CV_CS = 4160;
constexpr int CV_UO = 4 * CV_CS;
constexpr int CV_R = CV_UO + 4576;
constexpr int CV_OV = 81920 - 8192;
constexpr int G2_A = 8192, G2_STAGE = 18432, G2_NKT = F_ / 64;
constexpr int G2_SS = 3 * G2_STAGE;
constexpr int G2_BS = 44032;
constexpr int G2_FL = 73600;
constexpr int K2_LDS = 81920;
typedef __attribute__((ext_vector_type(2))) unsigned short u16x2;

struct CvKv { u32x4 kk; u32x2 vv; };
struct CvK { u32x2 lo; u32x4 hi; };
struct CvW { unsigned w01, w23, dpair; };
__device__ __forceinline__ void cv_ldkv(CvKv& in, const int d, const int t0, const u16* __restrict__ trT) {
  const u16* kd = trT + d * S_ + t0;
  in.kk = *(const u32x4*)(kd - (t0 > 0 ? 4 : 0));
  in.vv = *(const u32x2*)(kd + D_ * S_);
}
__device__ __forceinline__ void cv_ldk(CvK& in, const int d, const int i, const u16* __restrict__ Kf) {
  const u16* Kd = Kf + d * S_ + 8 * i;
  in.lo = *(const u32x2*)(Kd - (i > 0 ? 4 : 0));
  in.hi = *(const u32x4*)(Kd);
}
__device__ __forceinline__ void cv_ldw(CvW& w, const int d, const u16* __restrict__ sw, const u16* __restrict__ Dv) {
  w.w01 = ((const unsigned*)sw)[2 * d];
  w.w23 = ((const unsigned*)sw)[2 * d + 1];
  w.dpair = ((const unsigned*)Dv)[d >> 1];
}
__device__ __forceinline__ u32x2 cv_kv(const CvKv& in, const CvW& cw, char* reg, const int d, const int t0) {
  const float w0 = bflo(cw.w01), w1 = bfhi(cw.w01), w2 = bflo(cw.w23), w3 = bfhi(cw.w23);
  const float dd = (d & 1) ? bfhi(cw.dpair) : bflo(cw.dpair);
  const unsigned k0 = t0 > 0 ? in.kk[0] : 0u, k1 = t0 > 0 ? in.kk[1] : 0u;
  const unsigned k2 = t0 > 0 ? in.kk[2] : in.kk[0], k3 = t0 > 0 ? in.kk[3] : in.kk[1];
  const float kk[7] = {bfhi(k0), bflo(k1), bfhi(k1), bflo(k2), bfhi(k2), bflo(k3), bfhi(k3)};
  const float vf[4] = {bflo(in.vv[0]), bfhi(in.vv[0]), bflo(in.vv[1]), bfhi(in.vv[1])};
  unsigned kvr[2];
  u32x2 o;
#pragma unroll
  for (int e = 0; e < 4; e += 2) {
    unsigned a = pack2(kk[e] * w3, kk[e + 1] * w3);
    unsigned b = pack2(kk[e + 1] * w2, kk[e + 2] * w2);
    a = pack2(bflo(a) + bflo(b), bfhi(a) + bfhi(b));
    b = pack2(kk[e + 2] * w1, kk[e + 3] * w1);
    a = pack2(bflo(a) + bflo(b), bfhi(a) + bfhi(b));
    b = pack2(kk[e + 3] * w0, kk[e + 4] * w0);
    a = pack2(bflo(a) + bflo(b), bfhi(a) + bfhi(b));
    const unsigned r = pack2(bfhi(a) * vf[e + 1], bflo(a) * vf[e]);
    kvr[e >> 1] = r;
    o[e >> 1] = pack2(bfhi(r) * dd, bflo(r) * dd);
  }
  *(u32x2*)(reg + CV_UO + 2 * (2044 - t0)) = (u32x2){kvr[1], kvr[0]};
  return o;
}
__device__ __forceinline__ void cv_build(const CvK& in, char* reg, const int i) {
  const unsigned l0 = i > 0 ? in.lo[0] : 0u, l1 = i > 0 ? in.lo[1] : 0u;
  const unsigned v2 = __builtin_amdgcn_alignbit(l1, l0, 16), v3 = __builtin_amdgcn_alignbit(in.hi[0], l1, 16);
  const unsigned v4 = __builtin_amdgcn_alignbit(in.hi[1], in.hi[0], 16), v5 = __builtin_amdgcn_alignbit(in.hi[2], in.hi[1], 16);
  const unsigned v6 = __builtin_amdgcn_alignbit(in.hi[3], in.hi[2], 16);
  char* p = reg + 16 * (i + 2);
  *(u32x4*)(p + 3 * CV_CS) = in.hi;
  *(u32x4*)(p) = (u32x4){v2, v3, v4, v5};
  *(u32x4*)(p + CV_CS) = (u32x4){l1, in.hi[0], in.hi[1], in.hi[2]};
  *(u32x4*)(p + 2 * CV_CS) = (u32x4){v3, v4, v5, v6};
  if (i < 38) {
    const int o = i < 8 ? CV_CS * (i >> 1) + 16 * (i & 1) : CV_UO + 4096 + 16 * (i - 8);
    *(u32x4*)(reg + o) = (u32x4){0, 0, 0, 0};
  }
}
__device__ __forceinline__ bf16x8 cv_ra(const char* p) {
  const s16x4 lo = *(const s16x4*)p, hi = *(const s16x4*)(p + 8);
  return __builtin_shufflevector(lo, hi, 0, 1, 2, 3, 4, 5, 6, 7);
}
#define SB0() __builtin_amdgcn_sched_barrier(0)
template <bool MID>
__device__ __forceinline__ void cv_unit(const char* reg, const int lane, const int q, f32x4* acc) {
  const int c = lane & 15, g = lane >> 4;
#pragma unroll
  for (int n = 0; n < 8; n++) acc[n] = (f32x4){0.f, 0.f, 0.f, 0.f};
  const char* ab = reg + CV_CS * (c & 3) + 128 * q + 16 * g + 8 * (c >> 2) + 8;
  const char* bb = reg + CV_UO + 4064 - 32 * c + 128 * q + 16 * g - 512 * 7;
  bf16x8 G[2][8], A[2][8];
  SB0();
  A[0][0] = cv_ra(ab);
#pragma unroll
  for (int nu = 7; nu >= 0; nu--) G[0][nu] = *(const bf16x8*)(bb + 512 * (7 - nu));
  A[0][1] = cv_ra(ab + 512);
#pragma unroll
  for (int s = 0; s < 2; s++) {
#pragma unroll
    for (int p = 0; p < 8; p++) {
      SB0();
      if (p + 2 < 8) A[s][p + 2] = cv_ra(ab + 64 * s + 512 * (p + 2));
      else if (s == 0) A[1][p - 6] = cv_ra(ab + 64 + 512 * (p - 6));
      if (s == 0 && p > 0) G[1][8 - p] = *(const bf16x8*)(bb + 64 + 512 * (p - 1));
      if (s == 1 && p == 0) G[1][0] = *(const bf16x8*)(bb + 64 + 512 * 7);
      SB0();
#pragma unroll
      for (int n = 7; n >= p; n--)
        acc[n] = __builtin_amdgcn_mfma_f32_16x16x32_bf16(A[s][p], G[s][n - p], acc[n], 0, 0, 0);
    }
    SB0();
    if (MID && s == 0) BAR();
  }
}
__device__ __forceinline__ char* cv_slot(char* reg, char* ov, const int k) { return k < 20 ? reg + 1024 * k : ov + 1024 * (k - 20); }
__device__ __forceinline__ void cv_zst(const f32x4 s, const u32x2 kd, u16* dst) {
  const unsigned y0 = pack2(s[0], s[1]), y1 = pack2(s[2], s[3]);
  const u16x2 cap = {0xfffe, 0xfffe};
  u32x2 ov;
  ov[0] = __builtin_bit_cast(unsigned, __builtin_elementwise_min(__builtin_bit_cast(u16x2, pack2(bflo(y0) + bflo(kd[0]), bfhi(y0) + bfhi(kd[0]))), cap));
  ov[1] = __builtin_bit_cast(unsigned, __builtin_elementwise_min(__builtin_bit_cast(u16x2, pack2(bflo(y1) + bflo(kd[1]), bfhi(y1) + bfhi(kd[1]))), cap));
  asm volatile("global_store_dwordx2 %0, %1, off sc1" ::"v"(dst), "v"(ov) : "memory");
}
template <int Q>
__device__ __forceinline__ void cv_wr6t(char* reg, char* ov, const int lane, const f32x4* acc, f32x4& k0, f32x4& k1) {
#pragma unroll
  for (int n = 0; n < 8; n++) {
    const int o = n >> 1;
    if (o != Q) *(f32x4*)(cv_slot(reg, ov, n * 3 + ((Q - o - 1) & 3)) + lane * 16) = acc[n];
  }
  k0 = acc[2 * Q];
  k1 = acc[2 * Q + 1];
}
__device__ __forceinline__ void cv_wr6(char* reg, char* ov, const int lane, const int q, const f32x4* acc, f32x4& k0, f32x4& k1) {
  if (q == 0) cv_wr6t<0>(reg, ov, lane, acc, k0, k1);
  else if (q == 1) cv_wr6t<1>(reg, ov, lane, acc, k0, k1);
  else if (q == 2) cv_wr6t<2>(reg, ov, lane, acc, k0, k1);
  else cv_wr6t<3>(reg, ov, lane, acc, k0, k1);
}
template <int Q>
__device__ __forceinline__ void cv_wr7t(char* r2, const int lane, const f32x4* acc, f32x4& kc) {
#pragma unroll
  for (int n = 0; n < 8; n++) {
    const int o = n >> 1;
    if (n & 1) *(f32x4*)(r2 + (7 * o + 3 + Q) * 1024 + lane * 16) = acc[n];
    else if (o != Q) *(f32x4*)(r2 + (7 * o + ((Q - o - 1) & 3)) * 1024 + lane * 16) = acc[n];
  }
  kc = acc[2 * Q];
}
__device__ __forceinline__ void cv_wr7(char* r2, const int lane, const int q, const f32x4* acc, f32x4& kc) {
  if (q == 0) cv_wr7t<0>(r2, lane, acc, kc);
  else if (q == 1) cv_wr7t<1>(r2, lane, acc, kc);
  else if (q == 2) cv_wr7t<2>(r2, lane, acc, kc);
  else cv_wr7t<3>(r2, lane, acc, kc);
}
__device__ __forceinline__ void cv_epi2(char* reg, char* ov, const int lane, const int q, f32x4 k0, f32x4 k1, const u32x2 kda,
                                        const u32x2 kdb, u16* dst) {
  f32x4 a[3], b[3];
#pragma unroll
  for (int j = 0; j < 3; j++) {
    a[j] = *(const f32x4*)(cv_slot(reg, ov, 6 * q + j) + lane * 16);
    b[j] = *(const f32x4*)(cv_slot(reg, ov, 6 * q + 3 + j) + lane * 16);
  }
  cv_zst((k0 + a[0]) + (a[1] + a[2]), kda, dst);
  cv_zst((k1 + b[0]) + (b[1] + b[2]), kdb, dst + 256);
}
template <bool THREE>
__device__ __forceinline__ void cv_run(char* smem, const int tid, const int lane, const int w8, const int d0, const int d1,
                                       const int d2, const u16* __restrict__ Kf, const u16* __restrict__ trT,
                                       const u16* __restrict__ sw, const u16* __restrict__ Dv, u16* __restrict__ zT
                                       ) {
  const int q = w8 & 3, grp = w8 >> 2, i = tid & 255;
  const int tl = 16 * (lane & 15) + 4 * (lane >> 4);
  const int ta = 512 * q + tl;
  const int tc = 512 * q + 256 * grp + tl;
  const int dm = grp ? d1 : d0;
  char* rm = smem + grp * CV_R;
  char* r2 = smem + 2 * CV_R;
  char* ov = smem + CV_OV + grp * 4096;
  CvK Km, K2;
  CvKv ia, ib, ic;
  CvW wm, w2;
  cv_ldw(wm, dm, sw, Dv);
  cv_ldk(Km, dm, i, Kf);
  cv_ldkv(ia, dm, ta, trT);
  cv_ldkv(ib, dm, ta + 256, trT);
  if (THREE) {
    cv_ldw(w2, d2, sw, Dv);
    cv_ldkv(ic, d2, tc, trT);
    if (grp) cv_ldk(K2, d2, i, Kf);
  }
  asm volatile("" ::: "memory");
  __builtin_amdgcn_s_setprio(PRIO_C1);
  STAMP2(1);
  cv_build(Km, rm, i);
  const u32x2 kda = cv_kv(ia, wm, rm, dm, ta);
  const u32x2 kdb = cv_kv(ib, wm, rm, dm, ta + 256);
  u32x2 kdc;
  if (THREE) kdc = cv_kv(ic, w2, r2, d2, tc);
  WAIT_L0();
  STAMP2(2);
  BAR();
  f32x4 acc[8], k0, k1;
  if (THREE) {
    if (grp) {
      cv_build(K2, r2, i);
      WAIT_L0();
      cv_unit<true>(rm, lane, q, acc);
      WAIT_L0();
      STAMP2(4);
      BAR();
      cv_wr6(rm, ov, lane, q, acc, k0, k1);
      WAIT_L0();
      STAMP2(5);
      BAR();
      cv_epi2(rm, ov, lane, q, k0, k1, kda, kdb, zT + dm * S_ + ta);
      const char* pp = r2 + (7 * q + 3) * 1024 + lane * 16;
      const f32x4 s = (*(const f32x4*)pp + *(const f32x4*)(pp + 1024)) + (*(const f32x4*)(pp + 2048) + *(const f32x4*)(pp + 3072));
      cv_zst(s, kdc, zT + d2 * S_ + tc);
    } else {
      cv_unit<false>(rm, lane, q, acc);
      WAIT_L0();
      STAMP2(3);
      BAR();
      cv_wr6(rm, ov, lane, q, acc, k0, k1);
      cv_unit<false>(r2, lane, q, acc);
      WAIT_L0();
      STAMP2(4);
      BAR();
      f32x4 kc;
      cv_wr7(r2, lane, q, acc, kc);
      cv_epi2(rm, ov, lane, q, k0, k1, kda, kdb, zT + dm * S_ + ta);
      WAIT_L0();
      STAMP2(5);
      BAR();
      const char* pp = r2 + 7 * q * 1024 + lane * 16;
      const f32x4 s = (kc + *(const f32x4*)pp) + (*(const f32x4*)(pp + 1024) + *(const f32x4*)(pp + 2048));
      cv_zst(s, kdc, zT + d2 * S_ + tc);
    }
  } else {
    cv_unit<false>(rm, lane, q, acc);
    WAIT_L0();
    STAMP2(3);
    BAR();
    cv_wr6(rm, ov, lane, q, acc, k0, k1);
    WAIT_L0();
    STAMP2(5);
    BAR();
    cv_epi2(rm, ov, lane, q, k0, k1, kda, kdb, zT + dm * S_ + ta);
  }
}
#define STVP

__device__ __forceinline__ u32x4 gload16_sc1(const void* p) {
  u32x4 r;
  asm volatile("global_load_dwordx4 %0, %1, off sc1" : "=v"(r) : "v"(p) : "memory");
  return r;
}
__device__ __forceinline__ unsigned pkmax(unsigned a, unsigned b) {
  return __builtin_bit_cast(unsigned, __builtin_elementwise_max(__builtin_bit_cast(u16x2, a), __builtin_bit_cast(u16x2, b)));
}
template <int NB>
__device__ __forceinline__ bool g2_bad(const u32x4* sv) {
  unsigned m = 0;
#pragma unroll
  for (int j = 0; j < NB; j++) m = pkmax(m, pkmax(pkmax(sv[j][0], sv[j][1]), pkmax(sv[j][2], sv[j][3])));
  const bool bad = ((m & 0xffffu) == 0xffffu) || ((m >> 16) == 0xffffu);
  return __builtin_amdgcn_ballot_w64(bad) != 0;
}
template <int NB>
__device__ __forceinline__ void g2_wait0(u32x4* sv) {
  if (NB == 3) asm volatile("s_waitcnt vmcnt(0)" : "+v"(sv[0]), "+v"(sv[1]), "+v"(sv[NB - 1])::"memory");
  else asm volatile("s_waitcnt vmcnt(0)" : "+v"(sv[0]), "+v"(sv[1])::"memory");
}
template <int NB>
__device__ __forceinline__ void g2_finish_z(char* smem, u32x4* sv, const u16* sp, const int w, const int pb, const int lane,
                                            const int spin_max) {
  g2_wait0<NB>(sv);
  bool bad = g2_bad<NB>(sv);
#pragma unroll 1
  for (int spin = 0; bad && spin < spin_max; spin++) {
    __builtin_amdgcn_s_sleep(4);
#pragma unroll
    for (int j = 0; j < NB; j++) sv[j] = gload16_sc1(sp + j * 8 * S_);
    g2_wait0<NB>(sv);
    bad = g2_bad<NB>(sv);
  }
#pragma unroll
  for (int j = 0; j < NB; j++) *(u32x4*)(smem + G2_SS + ((pb + j) * 8 + (lane >> 3)) * 144 + (lane & 7) * 16) = sv[j];
  if (lane == 0) *(unsigned*)(smem + G2_FL + 4 * w) = bad ? 1u : 0u;
}

__device__ __forceinline__ u32x4 ln8(const u32x4 xv, const float m, float sd) {
  asm volatile("" : "+v"(sd));
  const float r = __builtin_amdgcn_rcpf(sd);
  u32x4 o;
#pragma unroll
  for (int i = 0; i < 4; i++) {
    const unsigned a = pack2(bflo(xv[i]) - m, bfhi(xv[i]) - m);
    const float a0 = bflo(a), a1 = bfhi(a);
    float q0 = a0 * r, q1 = a1 * r;
    q0 = fmaf(fmaf(-q0, sd, a0), r, q0);
    q1 = fmaf(fmaf(-q1, sd, a1), r, q1);
    o[i] = pack2(q0, q1);
  }
  return o;
}
__device__ __forceinline__ u32x2 gload8(const void* p) {
  u32x2 r;
  asm volatile("global_load_dwordx2 %0, %1, off" : "=v"(r) : "v"(p) : "memory");
  return r;
}
template <int NB>
__device__ __forceinline__ void g2_producer(char* smem, const int w, const int lane, const int m0, const int nt,
                                            const u16* __restrict__ h, const u16* __restrict__ wdp,
                                            const u16* __restrict__ zT, const u16* xa, const u16* xb, const float* sta,
                                            const float* stb, u32x4& xv0, u32x4& xv1, u32x4& xn0, u32x4& xn1) {
  constexpr int NP = 2 + NB;
  const int pb = (NB == 3) ? 3 * w : 2 * w + 2;
  unsigned asrc[2];
#pragma unroll
  for (int i = 0; i < 2; i++) {
    int row = (2 * w + i) * 8 + (lane >> 3), chunk = (lane & 7) ^ ((row >> 1) & 7);
    asrc[i] = ((m0 + row) * F_ + chunk * 8) * 2;
  }
  const unsigned bsrc = (unsigned)(nt * F_ * 160 + pb * 1024 + lane * 16);
  const unsigned sm0 = lds_off(smem);
  auto issue = [&](int kt) {
    const unsigned base = sm0 + (kt & 3) * G2_STAGE;
#pragma unroll
    for (int i = 0; i < 2; i++) glds16(asrc[i], h + kt * 64, base + (2 * w + i) * 1024);
#pragma unroll
    for (int i = 0; i < NB; i++) glds16(bsrc, wdp + kt * 5120 + i * 512, base + G2_A + (pb + i) * 1024);
  };
  u32x4 xa_ = gload16(xa), xb_ = xa_;
  u32x2 sa = gload8(sta), sb = sa;
  if (NB == 3) { xb_ = gload16(xb); sb = gload8(stb); }
  issue(0); issue(1); issue(2); issue(3);
  if (NB == 3) asm volatile("s_waitcnt vmcnt(%4)" : "+v"(xa_), "+v"(sa), "+v"(xb_), "+v"(sb) : "n"(3 * NP) : "memory");
  else asm volatile("s_waitcnt vmcnt(%2)" : "+v"(xa_), "+v"(sa) : "n"(3 * NP) : "memory");
  BAR();
  xv0 = xa_;
  xn0 = ln8(xa_, __uint_as_float(sa[0]), __uint_as_float(sa[1]));
  if (NB == 3) { xv1 = xb_; xn1 = ln8(xb_, __uint_as_float(sb[0]), __uint_as_float(sb[1])); }
  else { xv1 = xa_; xn1 = xn0; }
  asm volatile("" : "+v"(xn0), "+v"(xn1));
#pragma unroll 1
  for (int kt = 0; kt < G2_NKT - 4; kt++) {
    waitv<2 * NP>();
    BAR();
    issue(kt + 4);
  }
  waitv<2 * NP>();
  BAR();
  waitv<NP>();
  BAR();
  u32x4 sv[NB];
  const u16* sp = zT + (nt * 80 + pb * 8 + (lane >> 3)) * S_ + m0 + (lane & 7) * 8;
#pragma unroll
  for (int j = 0; j < NB; j++) sv[j] = gload16_sc1(sp + j * 8 * S_);
  waitv<NB>();
  BAR();
  BAR();
  g2_finish_z<NB>(smem, sv, sp, w, pb, lane, SPIN_MAX);
}
template <int NB>
__device__ __forceinline__ void g2_reload_z(char* smem, const int w, const int lane, const int m0, const int nt,
                                            const u16* __restrict__ zT) {
  const int pb = (NB == 3) ? 3 * w : 2 * w + 2;
  u32x4 sv[NB];
  const u16* sp = zT + (nt * 80 + pb * 8 + (lane >> 3)) * S_ + m0 + (lane & 7) * 8;
#pragma unroll
  for (int j = 0; j < NB; j++) sv[j] = gload16_sc1(sp + j * 8 * S_);
  g2_finish_z<NB>(smem, sv, sp, w, pb, lane, 0);
}
__device__ __forceinline__ unsigned fin2(const unsigned xv, const unsigned xn, const unsigned z, const unsigned b) {
  const unsigned a = pack2(bflo(xn) * bflo(z), bfhi(xn) * bfhi(z));
  const unsigned s = pack2(bflo(xv) + bflo(a), bfhi(xv) + bfhi(a));
  return pack2(bflo(s) + bflo(b), bfhi(s) + bfhi(b));
}

__global__ __launch_bounds__(512, 4) void k2(const u16* __restrict__ h, const u16* __restrict__ wdp,
                                             const u16* __restrict__ trT, const u16* __restrict__ Kf,
                                             const u16* __restrict__ sw, const u16* __restrict__ Dv,
                                             const float* __restrict__ stats, u16* __restrict__ zT,
                                             const u16* __restrict__ x, u16* __restrict__ out) {
  extern __shared__ __attribute__((aligned(16))) char smem[];
  const int tid = threadIdx.x, lane = tid & 63;
  const int w8 = __builtin_amdgcn_readfirstlane(tid >> 6);
  const int bx = blockIdx.x;
  STAMP_DECL
  STAMP2(0); STAMPR2(30);
  if (bx >= 256) {
    __builtin_amdgcn_s_setprio(PRIO_C0);
    const int i = bx - 256;
    if (i < 128) cv_run<true>(smem, tid, lane, w8, i, i + 256, i + 512, Kf, trT, sw, Dv, zT STVP);
    else cv_run<false>(smem, tid, lane, w8, i, i + 256, 0, Kf, trT, sw, Dv, zT STVP);
    STAMP2(9);
    STAMP2(10); STAMPR2(31);
    STAMP_OUT2;
    return;
  }
  __builtin_amdgcn_s_setprio(PRIO_G);
  const int xcd = bx & 7, idx = bx >> 3;
  const int mt = (xcd & 3) * 8 + (idx & 7), nt = (xcd >> 2) * 4 + (idx >> 3);
  const int m0 = mt * 64, n0 = nt * 80;
  const int i16 = lane & 15, g = lane >> 4;
  const int ea0 = w8 & 3, ec0 = 4 * (w8 >> 2) + g;
  const int ea1 = (2 * w8 + (g >> 1)) & 3, ec1 = 8 + (g & 1);
  const bool two = (w8 == 4) || (w8 == 5);
  u32x4 xv0, xn0, xv1, xn1;
  const int ta = m0 + 16 * ea0 + i16, tb = m0 + 16 * ea1 + i16;
  if (w8 >= 4) {
    const u16* xa = x + ta * D_ + n0 + 8 * ec0;
    const u16* xb = x + tb * D_ + n0 + 8 * ec1;
    if (w8 < 6) g2_producer<3>(smem, w8 - 4, lane, m0, nt, h, wdp, zT, xa, xb, stats + 2 * ta, stats + 2 * tb, xv0, xv1, xn0, xn1);
    else g2_producer<2>(smem, w8 - 4, lane, m0, nt, h, wdp, zT, xa, xb, stats + 2 * ta, stats + 2 * tb, xv0, xv1, xn0, xn1);
  } else {
    xv0 = *(const u32x4*)(x + ta * D_ + n0 + 8 * ec0);
    const f32x2 sa = *(const f32x2*)(stats + 2 * ta);
    xn0 = ln8(xv0, sa[0], sa[1]);
    asm volatile("" : "+v"(xn0));
    xn1 = xn0;
    xv1 = xv0;
    const int wr = w8 & 1, kh = w8 >> 1;
    const int r16 = lane & 15, q = r16 >> 2, p = lane & 3;
    int aoff[2];
#pragma unroll
    for (int rt = 0; rt < 2; rt++) {
      int row = wr * 32 + rt * 16 + r16, chunk = kh * 4 + g;
      aoff[rt] = (row * 8 + (chunk ^ ((row >> 1) & 7))) * 16;
    }
    const int boff = G2_A + (32 * kh + 16 * (g >> 1) + 4 * (g & 1) + q) * 160 + p * 8;
    f32x4 acc[2][5];
#pragma unroll
    for (int i = 0; i < 2; i++)
#pragma unroll
      for (int j = 0; j < 5; j++) acc[i][j] = (f32x4){0.f, 0.f, 0.f, 0.f};
    bf16x8 af0[2], bf0[5], af1[2], bf1[5];
    auto rd = [&](int kt, bf16x8* af, bf16x8* bfr) {
      const char* base = smem + (kt & 3) * G2_STAGE;
#pragma unroll
      for (int rt = 0; rt < 2; rt++) af[rt] = *(const bf16x8*)(base + aoff[rt]);
#pragma unroll
      for (int tt = 0; tt < 5; tt++) {
        s16x4 lo = trread(base + boff + tt * 32);
        s16x4 hi = trread(base + boff + tt * 32 + 1280);
        bfr[tt] = __builtin_shufflevector(lo, hi, 0, 1, 2, 3, 4, 5, 6, 7);
      }
    };
    auto mm = [&](bf16x8* af, bf16x8* bfr) {
#pragma unroll
      for (int tt = 0; tt < 5; tt++)
#pragma unroll
        for (int rt = 0; rt < 2; rt++)
          acc[rt][tt] = __builtin_amdgcn_mfma_f32_16x16x32_bf16(bfr[tt], af[rt], acc[rt][tt], 0, 0, 0);
    };
    auto sched = [&]() {
#pragma unroll
      for (int i = 0; i < 6; i++) { SGB(0x100, 2); SGB(0x8, 1); }
      SGB(0x8, 4);
    };
    BAR();
    STAMP2(1);
    rd(0, af0, bf0);
    WAIT_L0();
#pragma unroll 1
    for (int kt = 0; kt < G2_NKT - 1; kt += 2) {
      BAR();
      __builtin_amdgcn_sched_barrier(0);
      rd(kt + 1, af1, bf1);
      mm(af0, bf0);
      sched();
      __builtin_amdgcn_sched_barrier(0);
      WAIT_L0();
      BAR();
      __builtin_amdgcn_sched_barrier(0);
      rd(kt + 2, af0, bf0);
      mm(af1, bf1);
      sched();
      __builtin_amdgcn_sched_barrier(0);
      WAIT_L0();
    }
    BAR();
    STAMP2(2);
    mm(af0, bf0);
    float* red = (float*)smem;
#pragma unroll
    for (int rt = 0; rt < 2; rt++)
#pragma unroll
      for (int tt = 0; tt < 5; tt++)
        *(f32x4*)(red + (kh * 64 + wr * 32 + rt * 16 + r16) * 84 + tt * 16 + 4 * g) = acc[rt][tt];
  }
  WAIT_L0();
  STAMP2(3);
  BAR();
  STAMP2(4);
  const u32x4 fl = *(const u32x4*)(smem + G2_FL);
  bool slow = (fl[0] | fl[1] | fl[2] | fl[3]) != 0;
  slow = __builtin_amdgcn_readfirstlane(slow);
  u32x4 bq[2];
#pragma unroll
  for (int r = 0; r < 2; r++) {
    if (r == 1 && !two) break;
    const int t = 16 * (r ? ea1 : ea0) + i16, c = r ? ec1 : ec0;
    const float* rp = (const float*)smem + t * 84 + 8 * c;
    const f32x4 b0 = *(const f32x4*)rp + *(const f32x4*)(rp + 64 * 84);
    const f32x4 b1 = *(const f32x4*)(rp + 4) + *(const f32x4*)(rp + 64 * 84 + 4);
    bq[r] = (u32x4){pack2(b0[0], b0[1]), pack2(b0[2], b0[3]), pack2(b1[0], b1[1]), pack2(b1[2], b1[3])};
  }
  if (slow) {
    *(u32x4*)(smem + G2_BS + tid * 16) = bq[0];
    if (two) *(u32x4*)(smem + G2_BS + (tid + 256) * 16) = bq[1];
    WAIT_L0();
    BAR();
#pragma unroll 1
    for (int dd = 0; dd < 80; dd += 2) {
      cv_run<false>(smem, tid, lane, w8, n0 + dd, n0 + dd + 1, 0, Kf, trT, sw, Dv, zT STVP);
      WAIT_L0();
      BAR();
    }
    WAIT_V(0);
    BAR();
    if (w8 >= 4) {
      if (w8 < 6) g2_reload_z<3>(smem, w8 - 4, lane, m0, nt, zT);
      else g2_reload_z<2>(smem, w8 - 4, lane, m0, nt, zT);
    }
    WAIT_L0();
    BAR();
    bq[0] = *(const u32x4*)(smem + G2_BS + tid * 16);
    bq[1] = *(const u32x4*)(smem + G2_BS + (two ? tid + 256 : tid) * 16);
    int l2 = threadIdx.x & 63;
    asm volatile("" : "+v"(l2));
    const int ta2 = m0 + 16 * ea0 + (l2 & 15), tb2 = m0 + 16 * ((2 * w8 + (l2 >> 5)) & 3) + (l2 & 15);
    xv0 = *(const u32x4*)(x + ta2 * D_ + n0 + 8 * (4 * (w8 >> 2) + (l2 >> 4)));
    xv1 = *(const u32x4*)(x + tb2 * D_ + n0 + 8 * (8 + ((l2 >> 4) & 1)));
    xn0 = ln8(xv0, stats[2 * ta2], stats[2 * ta2 + 1]);
    xn1 = ln8(xv1, stats[2 * tb2], stats[2 * tb2 + 1]);
    STAMP2(7);
  }
#pragma unroll
  for (int r = 0; r < 2; r++) {
    if (r == 1 && !two) break;
    const int a = r ? ea1 : ea0, c = r ? ec1 : ec0;
    const int t = 16 * a + i16;
    const char* sp = smem + G2_SS + (8 * c + (i16 >> 2)) * 144 + 32 * a + 8 * (i16 & 3);
    const u32x2 zl = __builtin_bit_cast(u32x2, trread(sp)), zh = __builtin_bit_cast(u32x2, trread(sp + 4 * 144));
    const u32x4 xv = r ? xv1 : xv0, xn = r ? xn1 : xn0;
    u32x4 o;
    o[0] = fin2(xv[0], xn[0], zl[0], bq[r][0]);
    o[1] = fin2(xv[1], xn[1], zl[1], bq[r][1]);
    o[2] = fin2(xv[2], xn[2], zh[0], bq[r][2]);
    o[3] = fin2(xv[3], xn[3], zh[1], bq[r][3]);
    *(u32x4*)(out + (m0 + t) * D_ + n0 + 8 * c) = o;
  }
  STAMP2(5);
  STAMP2(6); STAMPR2(31);
  STAMP_OUT2;
}

extern "C" void kernel_launch(void* const* d_in, const int* in_sizes, int n_in, void* d_out, int out_size,
                              void* d_ws, size_t ws_size, hipStream_t stream) {
  const u16* x = (const u16*)d_in[0];
  const u16* k = (const u16*)d_in[1];
  const u16* v = (const u16*)d_in[2];
  const u16* sw = (const u16*)d_in[3];
  const u16* Kf = (const u16*)d_in[4];
  const u16* Dv = (const u16*)d_in[5];
  const u16* wg = (const u16*)d_in[7];
  const u16* wu = (const u16*)d_in[8];
  const u16* wd = (const u16*)d_in[9];
  u16* out = (u16*)d_out;
  char* ws = (char*)d_ws;
  u16* wdp = (u16*)(ws);
  float* stats = (float*)(ws + (3 << 20));
  u16* zT = (u16*)(ws + (4 << 20));
  u16* h = (u16*)(ws + (8 << 20));
  u16* trT = (u16*)(ws + (16 << 20));
  (void)hipFuncSetAttribute((const void*)k1, hipFuncAttributeMaxDynamicSharedMemorySize, G1_LDS);
  (void)hipFuncSetAttribute((const void*)k2, hipFuncAttributeMaxDynamicSharedMemorySize, K2_LDS);
#define ONLY 15
  if (ONLY & 2) k1<<<256, 512, G1_LDS, stream>>>(x, k, v, wg, wu, wd, h, trT, wdp, stats, zT, Kf);
  if (ONLY & 8) k2<<<512, 512, K2_LDS, stream>>>(h, wdp, trT, Kf, sw, Dv, stats, zT, x, out);
}
```

```cpp
#include <hip/hip_runtime.h>
#include <hip/hip_bf16.h>
#include <stdint.h>

typedef unsigned short u16;
typedef __attribute__((ext_vector_type(8))) short bf16x8;
typedef __attribute__((ext_vector_type(4))) short s16x4;
typedef __attribute__((ext_vector_type(4))) float f32x4;
typedef __attribute__((ext_vector_type(2))) float f32x2;
typedef __attribute__((ext_vector_type(4))) unsigned u32x4;
typedef __attribute__((ext_vector_type(2))) unsigned u32x2;
typedef __attribute__((ext_vector_type(2))) __bf16 bf16x2_t;
#define LDS_AS __attribute__((address_space(3)))

#define ABL 0
constexpr int S_ = 2048, D_ = 640, F_ = 1728;

__device__ __forceinline__ float bflo(unsigned v) { return __uint_as_float(v << 16); }
__device__ __forceinline__ float bfhi(unsigned v) { return __uint_as_float(v & 0xffff0000u); }
__device__ __forceinline__ unsigned pack2(float a, float b) {
  f32x2 v = {a, b};
  bf16x2_t r = __builtin_convertvector(v, bf16x2_t);
  return __builtin_bit_cast(unsigned, r);
}
__device__ __forceinline__ float rbf(float a) { __bf16 r = (__bf16)a; return (float)r; }
__device__ __forceinline__ float bf1(u16 v) { return __uint_as_float((unsigned)v << 16); }

__device__ __forceinline__ void glds16(unsigned voff, const void* sbase, unsigned lds_addr) {
  asm volatile("s_mov_b32 m0, %2\n\ts_nop 0\n\tglobal_load_lds_dwordx4 %0, %1" ::"v"(voff), "s"(sbase), "s"(lds_addr) : "memory");
}
__device__ __forceinline__ void glds16c(unsigned voff, const void* sbase, unsigned lds_addr) {
  asm volatile("s_mov_b32 m0, %2\n\ts_nop 0\n\tglobal_load_lds_dwordx4 %0, %1 sc1" ::"v"(voff), "s"(sbase), "s"(lds_addr) : "memory");
}
__device__ __forceinline__ void glds16v(const void* vptr, unsigned lds_addr) {
  asm volatile("s_mov_b32 m0, %1\n\ts_nop 0\n\tglobal_load_lds_dwordx4 %0, off" ::"v"(vptr), "s"(lds_addr) : "memory");
}
__device__ __forceinline__ void gtouch(unsigned voff, const void* sbase, unsigned lds_addr) {
  asm volatile("s_mov_b32 m0, %2\n\ts_nop 0\n\tglobal_load_lds_dword %0, %1" ::"v"(voff), "s"(sbase), "s"(lds_addr) : "memory");
}
__device__ __forceinline__ void glds4c(unsigned voff, const void* sbase, unsigned lds_addr) {
  asm volatile("s_mov_b32 m0, %2\n\ts_nop 0\n\tglobal_load_lds_dword %0, %1 sc1" ::"v"(voff), "s"(sbase), "s"(lds_addr) : "memory");
}
__device__ __forceinline__ u32x4 gload16(const void* p) {
  u32x4 r;
  asm volatile("global_load_dwordx4 %0, %1, off" : "=v"(r) : "v"(p) : "memory");
  return r;
}
__device__ __forceinline__ u32x2 gload8(const void* p) {
  u32x2 r;
  asm volatile("global_load_dwordx2 %0, %1, off" : "=v"(r) : "v"(p) : "memory");
  return r;
}
__device__ __forceinline__ void gst16c(unsigned voff, void* sbase, const u32x4 v) {
  asm volatile("global_store_dwordx4 %0, %1, %2 sc1\n\ts_nop 1" ::"v"(voff), "v"(v), "s"(sbase) : "memory");
}
__device__ __forceinline__ void gst8c(unsigned voff, void* sbase, const u32x2 v) {
  asm volatile("global_store_dwordx2 %0, %1, %2 sc1" ::"v"(voff), "v"(v), "s"(sbase) : "memory");
}
__device__ __forceinline__ void gst4c(unsigned voff, void* sbase, const unsigned v) {
  asm volatile("global_store_dword %0, %1, %2 sc1" ::"v"(voff), "v"(v), "s"(sbase) : "memory");
}
__device__ __forceinline__ unsigned lds_off(const void* p) { return (unsigned)(uintptr_t)(const LDS_AS char*)p; }
__device__ __forceinline__ s16x4 trread(const char* lds) {
  return __builtin_amdgcn_ds_read_tr16_b64_v4i16((LDS_AS s16x4*)lds);
}
#define WAIT_V(n) asm volatile("s_waitcnt vmcnt(" #n ")" ::: "memory")
#define WAIT_L0() asm volatile("s_waitcnt lgkmcnt(0)" ::: "memory")

#define STAMPS 0
#define SPIN_MAX 1500
#define STAMP_DECL
#define STAMP_OUT
#define STAMP(i)
#define STAMPR(i)
#define SGB(m, n) __builtin_amdgcn_sched_group_barrier(m, n, 0)
#define SB0() __builtin_amdgcn_sched_barrier(0)
#define BAR() asm volatile("s_barrier" ::: "memory")
#define MFMA16(a, b, c) __builtin_amdgcn_mfma_f32_16x16x32_bf16(a, b, c, 0, 0, 0)
template <int N> __device__ __forceinline__ void waitv() { asm volatile("s_waitcnt vmcnt(%0)" ::"n"(N) : "memory"); }
extern "C" __device__ unsigned long long cw_dispatch_id() __asm("llvm.amdgcn.dispatch.id");

constexpr int KP = 1792;
constexpr unsigned WS_WDP = 0;
constexpr unsigned WS_F1 = 3u << 20, WS_F2 = WS_F1 + 4096, WS_F3 = WS_F1 + 8192;
constexpr unsigned WS_Z = 4u << 20;
constexpr unsigned WS_H = 8u << 20;
constexpr unsigned WS_KV = 16u << 20;
constexpr int G1_A = 16384, G1_B = 28672, G1_STAGE = G1_A + G1_B;
constexpr int L_SP = 3 * G1_STAGE;
constexpr int KVS_V = 11264, KVS_W = 21504;
constexpr int CV_CS = 4160;
constexpr int CV_UO = 4 * CV_CS;
constexpr int CV_R = CV_UO + 4576;
constexpr int CV_R0 = G1_STAGE;
constexpr int CV_P = 2 * G1_STAGE;
constexpr int G2_A = 16384, G2_STAGE = 36864, G2_NKT = KP / 128;
constexpr int G2_ZS = 4 * G2_STAGE;
constexpr int L_ST = 160000;
constexpr int L_RQ = L_ST, L_RS = L_ST + 512, L_CS = L_ST + 1024, L_MS = L_ST + 1920;
constexpr int L_CT = L_ST + 2944;
constexpr int L_F3 = L_CT + 128;
constexpr int L_DUMP = L_F3 + 256;
constexpr int K_LDS = L_DUMP + 256;
enum { C_STAT, C_LOOP, C_BUILD, C_PA, C_PB, C_P2, C_Z, C_H, C_BAD, C_GO, C_ZL };

__device__ __forceinline__ unsigned ct_add(char* smem, const int c, const int lane) {
  unsigned r = 0;
  if (lane == 0) r = __hip_atomic_fetch_add((LDS_AS unsigned*)(smem + L_CT + 4 * c), 1u, __ATOMIC_RELAXED, __HIP_MEMORY_SCOPE_WORKGROUP);
  return __builtin_amdgcn_readfirstlane(r);
}
__device__ __forceinline__ unsigned ct_get(char* smem, const int c) {
  return __builtin_amdgcn_readfirstlane(*(volatile LDS_AS unsigned*)(smem + L_CT + 4 * c));
}
__device__ __forceinline__ void ct_wait(char* smem, const int c, const unsigned n) {
  while (ct_get(smem, c) < n) __builtin_amdgcn_s_sleep(1);
  asm volatile("" ::: "memory");
}
struct CvK { u32x2 lo; u32x4 hi; };
__device__ __forceinline__ void cv_ldk(CvK& in, const int d, const int i, const u16* __restrict__ Kf) {
  const u16* Kd = Kf + d * S_ + 8 * i;
  in.lo = gload8(Kd - (i > 0 ? 4 : 0));
  in.hi = gload16(Kd);
}
__device__ __forceinline__ void cv_build(const CvK& in, char* reg, const int i) {
  const unsigned l0 = i > 0 ? in.lo[0] : 0u, l1 = i > 0 ? in.lo[1] : 0u;
  const unsigned v2 = __builtin_amdgcn_alignbit(l1, l0, 16), v3 = __builtin_amdgcn_alignbit(in.hi[0], l1, 16);
  const unsigned v4 = __builtin_amdgcn_alignbit(in.hi[1], in.hi[0], 16), v5 = __builtin_amdgcn_alignbit(in.hi[2], in.hi[1], 16);
  const unsigned v6 = __builtin_amdgcn_alignbit(in.hi[3], in.hi[2], 16);
  char* p = reg + 16 * (i + 2);
  *(u32x4*)(p + 3 * CV_CS) = in.hi;
  *(u32x4*)(p) = (u32x4){v2, v3, v4, v5};
  *(u32x4*)(p + CV_CS) = (u32x4){l1, in.hi[0], in.hi[1], in.hi[2]};
  *(u32x4*)(p + 2 * CV_CS) = (u32x4){v3, v4, v5, v6};
  if (i < 38) {
    const int o = i < 8 ? CV_CS * (i >> 1) + 16 * (i & 1) : CV_UO + 4096 + 16 * (i - 8);
    *(u32x4*)(reg + o) = (u32x4){0, 0, 0, 0};
  }
}
__device__ __forceinline__ bf16x8 cv_ra(const char* p) {
  const s16x4 lo = *(const s16x4*)p, hi = *(const s16x4*)(p + 8);
  return __builtin_shufflevector(lo, hi, 0, 1, 2, 3, 4, 5, 6, 7);
}
__device__ __forceinline__ void cv_half(const char* reg, const int lane, const int s0, f32x4* acc) {
  const int c = lane & 15, g = lane >> 4;
#pragma unroll
  for (int n = 0; n < 8; n++) acc[n] = (f32x4){0.f, 0.f, 0.f, 0.f};
  const char* ab = reg + CV_CS * (c & 3) + 64 * s0 + 16 * g + 8 * (c >> 2) + 8;
  const char* bb = reg + CV_UO + 4064 - 32 * c + 64 * s0 + 16 * g - 512 * 7;
  bf16x8 G[2][8], A[2][8];
  SB0();
  A[0][0] = cv_ra(ab);
#pragma unroll
  for (int nu = 7; nu >= 0; nu--) G[0][nu] = *(const bf16x8*)(bb + 512 * (7 - nu));
  A[0][1] = cv_ra(ab + 512);
#pragma unroll
  for (int s = 0; s < 4; s++) {
    const int cu = s & 1, nx = cu ^ 1;
#pragma unroll
    for (int p = 0; p < 8; p++) {
      SB0();
      if (p + 2 < 8) A[cu][p + 2] = cv_ra(ab + 64 * s + 512 * (p + 2));
      else if (s < 3) A[nx][p - 6] = cv_ra(ab + 64 * (s + 1) + 512 * (p - 6));
      if (s < 3 && p > 0) G[nx][8 - p] = *(const bf16x8*)(bb + 64 * (s + 1) + 512 * (p - 1));
      if (s > 0 && p == 0) G[cu][0] = *(const bf16x8*)(bb + 64 * s + 512 * 7);
      SB0();
#pragma unroll
      for (int n = 7; n >= p; n--) acc[n] = MFMA16(A[cu][p], G[cu][n - p], acc[n]);
    }
    SB0();
  }
}
template <int E>
__device__ __forceinline__ void cv_part(const char* reg, const int lane, const int sl, f32x4* acc) {
  const int c = lane & 15, g = lane >> 4;
  const char* ab = reg + CV_CS * (c & 3) + 64 * sl + 16 * g + 8 * (c >> 2) + 8;
  const char* bb = reg + CV_UO + 4064 - 32 * c + 64 * sl + 16 * g - 512 * 7;
  constexpr int NT = E ? 6 : 8;
  bf16x8 G[2][NT], A[2][NT];
#pragma unroll
  for (int s = 0; s < 2; s++)
#pragma unroll
    for (int p = 0; p < NT; p++) {
      A[s][p] = cv_ra(ab + 64 * s + 512 * p);
      G[s][NT - 1 - p] = *(const bf16x8*)(bb + 64 * s + 512 * (8 - NT + p));
    }
#pragma unroll
  for (int n = 0; n < 4; n++) acc[n] = (f32x4){0.f, 0.f, 0.f, 0.f};
#pragma unroll
  for (int s = 0; s < 2; s++)
#pragma unroll
    for (int p = 0; p < NT; p++)
#pragma unroll
      for (int b = 0; b < 4; b++) {
        const int n = E ? 5 - b : (b < 2 ? 7 - b : 3 - b);
        if (n >= p) acc[b] = MFMA16(A[s][p], G[s][n - p], acc[b]);
      }
}
__device__ __forceinline__ u32x2 cv_z(const f32x4 s, const char* kvp, const float dd) {
  const u32x2 kv = *(const u32x2*)kvp;
  const unsigned y0 = pack2(s[0], s[1]), y1 = pack2(s[2], s[3]);
  const unsigned k0 = pack2(bfhi(kv[1]) * dd, bflo(kv[1]) * dd), k1 = pack2(bfhi(kv[0]) * dd, bflo(kv[0]) * dd);
  return (u32x2){pack2(bflo(y0) + bflo(k0), bfhi(y0) + bfhi(k0)), pack2(bflo(y1) + bflo(k1), bfhi(y1) + bfhi(k1))};
}
__device__ __forceinline__ float ld_D(const u16* __restrict__ Dv, const int d) {
  const unsigned p = ((const unsigned*)Dv)[d >> 1];
  return (d & 1) ? bfhi(p) : bflo(p);
}
__device__ __forceinline__ float rb16(const float a) { return __uint_as_float(pack2(0.f, a)); }
__device__ __forceinline__ u32x4 kv8(const u32x2 kp, const u32x2 kl, const u32x2 kh, const u32x2 vl, const u32x2 vh, const u32x2 wv) {
  const float w0 = bflo(wv[0]), w1 = bfhi(wv[0]), w2 = bflo(wv[1]), w3 = bfhi(wv[1]);
  const float kk[11] = {bfhi(kp[0]), bflo(kp[1]), bfhi(kp[1]), bflo(kl[0]), bfhi(kl[0]), bflo(kl[1]),
                        bfhi(kl[1]), bflo(kh[0]), bfhi(kh[0]), bflo(kh[1]), bfhi(kh[1])};
  const float vf[8] = {bflo(vl[0]), bfhi(vl[0]), bflo(vl[1]), bfhi(vl[1]), bflo(vh[0]), bfhi(vh[0]), bflo(vh[1]), bfhi(vh[1])};
  float a[8];
#define KV_FMA(d, x, y, z) asm("v_fma_f32 %0, %1, %2, %3" : "=v"(d) : "v"(x), "v"(y), "v"(z))
#define KV_MUL(d, x, y) asm("v_mul_f32 %0, %1, %2" : "=v"(d) : "v"(x), "v"(y))
#pragma unroll
  for (int e = 0; e < 8; e++) KV_MUL(a[e], kk[e], w3);
#pragma unroll
  for (int e = 0; e < 8; e++) KV_FMA(a[e], kk[e + 1], w2, a[e]);
#pragma unroll
  for (int e = 0; e < 8; e++) KV_FMA(a[e], kk[e + 2], w1, a[e]);
#pragma unroll
  for (int e = 0; e < 8; e++) KV_FMA(a[e], kk[e + 3], w0, a[e]);
#pragma unroll
  for (int e = 0; e < 8; e++) a[e] = rb16(a[e]);
#pragma unroll
  for (int e = 0; e < 8; e++) KV_MUL(a[e], a[e], vf[e]);
  u32x4 kvr;
#pragma unroll
  for (int m = 0; m < 4; m++) kvr[3 - m] = pack2(a[2 * m + 1], a[2 * m]);
  return kvr;
}
__device__ __forceinline__ u32x4 ln8(const u32x4 xv, const float m, float sd) {
  asm volatile("" : "+v"(sd));
  const float r = __builtin_amdgcn_rcpf(sd);
  u32x4 o;
#pragma unroll
  for (int i = 0; i < 4; i++) {
    const unsigned a = pack2(bflo(xv[i]) - m, bfhi(xv[i]) - m);
    const float a0 = bflo(a), a1 = bfhi(a);
    float q0 = a0 * r, q1 = a1 * r;
    q0 = fmaf(fmaf(-q0, sd, a0), r, q0);
    q1 = fmaf(fmaf(-q1, sd, a1), r, q1);
    o[i] = pack2(q0, q1);
  }
  return o;
}
__device__ __forceinline__ unsigned sum2(const unsigned xv, const unsigned xn, const unsigned z) {
  const unsigned a = pack2(bflo(xn) * bflo(z), bfhi(xn) * bfhi(z));
  return pack2(bflo(xv) + bflo(a), bfhi(xv) + bfhi(a));
}
__device__ __forceinline__ int g2_row(const int r, const int w, const int lane) {
  const int g = lane >> 4;
  return 16 * (r < 2 ? 2 * r + (g >> 1) : 2 * (w & 1) + (g & 1)) + (lane & 15);
}
__device__ __forceinline__ int g2_pc(const int r, const int w, const int lane) { return r < 2 ? 2 * w + ((lane >> 4) & 1) : 8 + (w >> 1); }
template <int NW>
__device__ __forceinline__ bool poll_flags(const char* ws, const unsigned voff, const unsigned ep) {
  bool ok = false;
#pragma unroll 1
  for (int spin = 0; spin < SPIN_MAX; spin++) {
    bool b;
    if (NW == 4) {
      u32x4 r;
      asm volatile("global_load_dwordx4 %0, %1, %2 sc1\n\ts_waitcnt vmcnt(0)" : "=v"(r) : "v"(voff), "s"(ws) : "memory");
      b = (r[0] != ep) | (r[1] != ep) | (r[2] != ep) | (r[3] != ep);
    } else {
      unsigned r;
      asm volatile("global_load_dword %0, %1, %2 sc1\n\ts_waitcnt vmcnt(0)" : "=v"(r) : "v"(voff), "s"(ws) : "memory");
      b = r != ep;
    }
    if (__builtin_amdgcn_ballot_w64(b) == 0) { ok = true; break; }
    __builtin_amdgcn_s_sleep(2);
  }
  return ok;
}
#define SYNC() do { asm volatile("s_waitcnt vmcnt(0) lgkmcnt(0)\n\ts_barrier" ::: "memory"); } while (0)
__device__ __forceinline__ void slow_tile(char* smem, const u16* __restrict__ x, const u16* __restrict__ kin,
                                          const u16* __restrict__ vin, const u16* __restrict__ sw, const u16* __restrict__ Kf,
                                          const u16* __restrict__ Dv, const u16* __restrict__ wg, const u16* __restrict__ wu,
                                          const u16* __restrict__ wd, u16* __restrict__ out, const int m0, const int n0) {
  int tid = threadIdx.x;
  asm volatile("" : "+v"(tid));
  const int lane = tid & 63, w8 = tid >> 6;
  u16* xn = (u16*)smem;
  u16* H = (u16*)(smem + 81920);
  float* kvs = (float*)(smem + 90112);
  float* red = (float*)(smem + 98304);
  u16* Z = (u16*)(smem + 100352);
  SYNC();
#pragma unroll 1
  for (int r = w8 * 8; r < w8 * 8 + 8; r++) {
    const u16* xr = x + (m0 + r) * D_;
    float s = 0.f;
    for (int j = lane; j < D_; j += 64) s += bf1(xr[j]);
    for (int o = 32; o >= 1; o >>= 1) s += __shfl_xor(s, o, 64);
    const float mean = s * (1.0f / D_);
    float q = 0.f;
    for (int j = lane; j < D_; j += 64) { const float e = bf1(xr[j]) - mean; q += e * e; }
    for (int o = 32; o >= 1; o >>= 1) q += __shfl_xor(q, o, 64);
    const float m = rbf(mean), sd = rbf(sqrtf(rbf(rbf(q * (1.0f / D_)) + 1e-5f)));
    for (int j = lane; j < D_; j += 64) xn[r * D_ + j] = (u16)(pack2(rbf(bf1(xr[j]) - m) / sd, 0.f) & 0xffffu);
  }
  SYNC();
  const int row = tid >> 3, sub = tid & 7;
  float acc[10];
#pragma unroll
  for (int c = 0; c < 10; c++) acc[c] = 0.f;
#pragma unroll 1
  for (int f0 = 0; f0 < F_; f0 += 64) {
    float g[8], u[8];
#pragma unroll
    for (int j = 0; j < 8; j++) { g[j] = 0.f; u[j] = 0.f; }
#pragma unroll 2
    for (int k = 0; k < D_; k++) {
      const float a = bf1(xn[row * D_ + k]);
      const u32x4 wa = *(const u32x4*)(wg + k * F_ + f0 + 8 * sub), wb = *(const u32x4*)(wu + k * F_ + f0 + 8 * sub);
#pragma unroll
      for (int j = 0; j < 4; j++) {
        g[2 * j] = fmaf(a, bflo(wa[j]), g[2 * j]); g[2 * j + 1] = fmaf(a, bfhi(wa[j]), g[2 * j + 1]);
        u[2 * j] = fmaf(a, bflo(wb[j]), u[2 * j]); u[2 * j + 1] = fmaf(a, bfhi(wb[j]), u[2 * j + 1]);
      }
    }
#pragma unroll
    for (int j = 0; j < 4; j++)
      *(unsigned*)(H + row * 64 + 8 * sub + 2 * j) = pack2(rbf(g[2 * j]) * rbf(u[2 * j]), rbf(g[2 * j + 1]) * rbf(u[2 * j + 1]));
    SYNC();
#pragma unroll 2
    for (int f = 0; f < 64; f++) {
      const float a = bf1(H[row * 64 + f]);
      const u16* wr = wd + (f0 + f) * D_ + n0 + 10 * sub;
#pragma unroll
      for (int c = 0; c < 5; c++) {
        const unsigned p = *(const unsigned*)(wr + 2 * c);
        acc[2 * c] = fmaf(a, bflo(p), acc[2 * c]);
        acc[2 * c + 1] = fmaf(a, bfhi(p), acc[2 * c + 1]);
      }
    }
    SYNC();
  }
#pragma unroll 1
  for (int c = 0; c < 80; c++) {
    const int d = n0 + c;
    for (int t = tid; t < m0 + 64; t += 512) {
      float a = 0.f;
#pragma unroll
      for (int j = 0; j < 4; j++) {
        const int ti = t + j - 3;
        a = fmaf(ti >= 0 ? bf1(kin[ti * D_ + d]) : 0.f, bf1(sw[4 * d + 3 - j]), a);
      }
      kvs[t] = rbf(rbf(a) * bf1(vin[t * D_ + d]));
    }
    SYNC();
    const int T = m0 + lane;
    float y = 0.f;
    for (int s = w8; s <= T; s += 8) y = fmaf(bf1(Kf[d * S_ + s]), kvs[T - s], y);
    red[w8 * 64 + lane] = y;
    SYNC();
    if (tid < 64) {
      float ys = 0.f;
#pragma unroll
      for (int j = 0; j < 8; j++) ys += red[j * 64 + lane];
      Z[lane * 80 + c] = (u16)(pack2(rbf(ys) + rbf(kvs[T] * bf1(Dv[d])), 0.f) & 0xffffu);
    }
    SYNC();
  }
#pragma unroll
  for (int c = 0; c < 10; c++) {
    const int col = 10 * sub + c, o = (m0 + row) * D_ + n0 + col;
    const float a = rbf(bf1(xn[row * D_ + n0 + col]) * bf1(Z[row * 80 + col]));
    out[o] = (u16)(pack2(rbf(bf1(x[o]) + a) + rbf(acc[c]), 0.f) & 0xffffu);
  }
}
#define T1N 3
#define TDELAY 0
#define PRIO_M 2
static __device__ __attribute__((noinline)) void cw_code_end() { asm volatile("s_nop 0"); }
extern "C" __global__ __launch_bounds__(512) void cw_all(const u16* __restrict__ x, const u16* __restrict__ wg,
                                                         const u16* __restrict__ wu, char* __restrict__ ws,
                                                         const u16* __restrict__ Kf, const u16* __restrict__ wd,
                                                         const u16* __restrict__ kin, const u16* __restrict__ vin,
                                                         const u16* __restrict__ sw, const u16* __restrict__ Dv,
                                                         u16* __restrict__ out) {
  extern __shared__ __attribute__((aligned(16))) char smem[];
  const int tid = threadIdx.x, lane = tid & 63;
  const int w8 = __builtin_amdgcn_readfirstlane(tid >> 6);
  const int bx = blockIdx.x, xcd = bx & 7, idx = bx >> 3;
  const int mp = (xcd & 1) * 8 + (idx & 7), nw = (xcd >> 1) * 4 + (idx >> 3);
  const int m0 = mp * 128, n0 = (108 * nw) & ~7;
  const int odd = nw & 1;
  constexpr int NKT = D_ / 64;
  const unsigned ep = (unsigned)cw_dispatch_id() * 2654435761u + 0x6a09e667u;
  const int kt0 = 64 * ((bx & 7) * 4 + ((bx >> 3) & 3)), kd0 = 80 * (bx >> 5);
  const int ce = bx & 1, cd2 = 5 * (bx >> 1) + 2, cd0 = cd2 - 2 + 3 * ce;
  const int mh = (xcd >> 1) & 1, nt = (xcd >> 2) * 4 + (idx >> 3);
  const int om0 = m0 + 64 * mh, on0 = nt * 80;
  const unsigned sm0 = lds_off(smem);
  const int g = lane >> 4, r16 = lane & 15;
  STAMP_DECL
  STAMP(0); STAMPR(30);
  u32x4 sq[3];
  if (w8 >= 4) {
    const int w = w8 - 4, pt = tid - 256;
    const bool tw = (w == 1) || (w == 2);
    const u16* tb = tw ? (w == 1 ? wg : wu) : x;
    unsigned tv2;
    {
      const int sl = tw ? lane / 7 : lane >> 3;
      const int ln = 7 * bx + lane % 7;
      const unsigned b0 = tw ? (ln < 1727 ? ln : 1727) * 128 : (8 * bx + (lane & 7)) * (D_ * 2);
      const unsigned sst = tw ? 64 * F_ * 2 : 128;
      gtouch(b0 + (1 + (sl < T1N - 1 ? sl : T1N - 1)) * sst, tb, sm0 + L_DUMP);
      tv2 = b0 + (T1N + 1 + (sl < 8 - T1N ? sl : 8 - T1N)) * sst;
    }
    unsigned asrc[4];
    const u16* bsrc[7];
#pragma unroll
    for (int i = 0; i < 4; i++) {
      int slot = (w * 4 + i) * 64 + lane, row = slot >> 3, chunk = (slot & 7) ^ ((row >> 1) & 7);
      asrc[i] = ((m0 + row) * D_ + chunk * 8) * 2;
    }
    {
      const u16* wb = ((lane & 1) ? wu : wg) + n0;
      const int q28 = (lane >= 28) + (lane >= 56);
      int kk = 16 * w + q28, p = lane - 28 * q28;
#pragma unroll
      for (int i = 0; i < 7; i++) {
        const int tau = (p >> 1) ^ ((kk >> 3) & 1);
        bsrc[i] = wb + (unsigned)(kk * F_ + 8 * tau);
        p += 8; kk += 2;
        if (p >= 28) { p -= 28; kk++; }
      }
    }
    auto issue = [&](int kt) {
      const unsigned base = sm0 + (kt % 3) * G1_STAGE;
#pragma unroll
      for (int i = 0; i < 4; i++) glds16(asrc[i], x + kt * 64, base + (w * 4 + i) * 1024);
#pragma unroll
      for (int i = 0; i < 7; i++) glds16v(bsrc[i] + kt * 64 * F_, base + G1_A + (w * 7 + i) * 1024);
    };
    issue(0);
    issue(1);
    waitv<11>();
    STAMP(1);
    BAR();
    issue(2);
    gtouch(tv2, tb, sm0 + L_DUMP);
    waitv<12>();
    BAR();
    STAMP(2);
    issue(3);
#pragma unroll
    for (int i = 0; i < 6; i++) {
      const int q = w * 6 + i;
      const u16* src;
      unsigned dst;
      if (q < 21) {
        const bool isk = q < 11;
        int c = (isk ? q : q - 11) * 64 + lane;
        c = c < 669 ? c : 669;
        int row = kt0 + c / 10 - (isk ? 3 : 0);
        row = row > 0 ? row : 0;
        src = (isk ? kin : vin) + (unsigned)(row * D_ + kd0 + 8 * (c % 10));
        dst = q * 1024;
      } else {
        src = sw + 4 * kd0 + 8 * (lane < 40 ? lane : 0);
        dst = KVS_W;
      }
      glds16v(src, sm0 + L_SP + dst);
    }
    waitv<17>();
    BAR();
    STAMP(3);
    issue(4);
    u32x4 pk[3];
    unsigned pdst[3];
    bool pz[3];
#pragma unroll
    for (int j = 0; j < 3; j++) {
      const int s0 = pt + 256 * j, s = s0 < 640 ? s0 : 0;
      const int r = s / 80, c = s % 80;
      const int P = (bx < 224 ? bx : 0) * 8 + r;
      const int kq = (P & ~15) | ((P & 4) << 1) | ((P & 8) >> 1) | (P & 3);
      const int tn = kq / 112, j2 = kq % 112;
      pz[j] = (tn & 1) ? j2 < 4 : j2 >= 108;
      const int f = pz[j] ? 0 : ((108 * tn) & ~7) + j2;
      pk[j] = gload16(wd + (unsigned)(f * D_ + c * 8));
      pdst[j] = WS_WDP + (((c / 10) * KP + P) * 10 + c % 10) * 16;
    }
    waitv<20>();
    BAR();
    STAMP(4);
    issue(5);
    waitv<14>();
    BAR();
    STAMP(5);
    issue(6);
    asm volatile("s_waitcnt vmcnt(22)" : "+v"(pk[0]), "+v"(pk[1]), "+v"(pk[2])::"memory");
#pragma unroll
    for (int j = 0; j < 3; j++)
      if (bx < 224 && (j < 2 || w < 2)) gst16c(pdst[j], ws, pz[j] ? (u32x4){0u, 0u, 0u, 0u} : pk[j]);
    const char* tl = smem + L_SP + (3 + 8 * g + (r16 >> 2)) * 160 + (lane & 3) * 8;
    auto kvjob = [&](int u) {
      const int th = u >= 5, dc = u - 5 * th;
      const char* p = tl + th * 32 * 160 + dc * 32;
      u32x2 kp = __builtin_bit_cast(u32x2, trread(p - 640));
      const u32x2 kl = __builtin_bit_cast(u32x2, trread(p)), kh = __builtin_bit_cast(u32x2, trread(p + 640));
      const u32x2 vl = __builtin_bit_cast(u32x2, trread(p + KVS_V - 480)), vh = __builtin_bit_cast(u32x2, trread(p + KVS_V + 160));
      const u32x2 wv = *(const u32x2*)(smem + L_SP + KVS_W + 8 * (16 * dc + r16));
      if (kt0 + th + g == 0) kp = (u32x2){0u, 0u};
      gst16c(WS_KV + ((kd0 + 16 * dc + r16) * S_ + 2040 - kt0 - 32 * th - 8 * g) * 2, ws, kv8(kp, kl, kh, vl, vh, wv));
    };
    kvjob(w);
    kvjob(w + 4);
    waitv<13>();
    BAR();
    STAMP(6);
    issue(7);
    if (w < 2) kvjob(w + 8);
    CvK Ka, Kb, Kc;
    cv_ldk(Ka, cd0, pt, Kf);
    cv_ldk(Kb, cd0 + 1, pt, Kf);
    cv_ldk(Kc, cd2, pt, Kf);
    waitv<19>();
    BAR();
    STAMP(7);
    issue(8);
    waitv<17>();
    BAR();
    STAMP(8);
    if (tid == 256) gst4c(WS_F1 + 4 * bx, ws, ep);
    issue(9);
    asm volatile("s_waitcnt vmcnt(0)" : "+v"(Ka.lo), "+v"(Ka.hi), "+v"(Kb.lo), "+v"(Kb.hi), "+v"(Kc.lo), "+v"(Kc.hi)::"memory");
    BAR();
    STAMP(9);
    cv_build(Ka, smem + CV_R0, pt);
    cv_build(Kb, smem + CV_R0 + CV_R, pt);
    cv_build(Kc, smem + L_SP, pt);
    const bool ok1 = poll_flags<4>(ws, WS_F1 + lane * 16, ep);
    STAMP(10);
    if (ok1) {
      const unsigned vo = WS_KV + w * 1024 + lane * 16;
      glds16c(vo, ws + cd0 * (S_ * 2), sm0 + CV_R0 + CV_UO + w * 1024);
      glds16c(vo, ws + (cd0 + 1) * (S_ * 2), sm0 + CV_R0 + CV_R + CV_UO + w * 1024);
      glds16c(vo, ws + cd2 * (S_ * 2), sm0 + L_SP + CV_UO + w * 1024);
    } else {
      ct_add(smem, C_BAD, lane);
    }
    asm volatile("s_waitcnt vmcnt(0) lgkmcnt(0)" ::: "memory");
    ct_add(smem, C_BUILD, lane);
    ct_wait(smem, C_BUILD, 4);
    STAMP(11);
    const bool bad1 = ct_get(smem, C_BAD) != 0;
    if (!bad1) {
      const int ch = w >> 1, hh = w & 1;
      const char* reg = smem + CV_R0 + ch * CV_R;
      f32x4 acc[8], kp[4];
      cv_half(reg, lane, 4 * hh, acc);
      STAMP(12);
      ct_wait(smem, C_LOOP, 4);
      char* pw = smem + CV_P + w * 4096 + lane * 16;
      if (hh) {
#pragma unroll
        for (int n = 0; n < 4; n++) { *(f32x4*)(pw + n * 1024) = acc[n]; kp[n] = acc[4 + n]; }
      } else {
#pragma unroll
        for (int n = 0; n < 4; n++) { *(f32x4*)(pw + n * 1024) = acc[4 + n]; kp[n] = acc[n]; }
      }
      WAIT_L0();
      ct_add(smem, C_PA + ch, lane);
      const float dd = ld_D(Dv, cd0 + ch);
      ct_wait(smem, C_PA + ch, 2);
      const char* pr = smem + CV_P + (w ^ 1) * 4096 + lane * 16;
      const int t0 = 1024 * hh + 16 * r16 + 4 * g;
      u32x2 z[4];
#pragma unroll
      for (int n = 0; n < 4; n++) z[n] = cv_z(kp[n] + *(const f32x4*)(pr + n * 1024), reg + CV_UO + 2 * (2044 - t0 - 256 * n), dd);
      const unsigned zo = WS_Z + ((cd0 + ch) * S_ + 1024 * hh + 256 * (g & 1) + 16 * r16 + 8 * (g >> 1)) * 2;
#pragma unroll
      for (int n = 0; n < 4; n += 2) {
        const u32x2 a = __builtin_amdgcn_permlane16_swap(z[n][0], z[n + 1][0], false, false);
        const u32x2 b = __builtin_amdgcn_permlane16_swap(z[n][1], z[n + 1][1], false, false);
        gst16c(zo + 512 * n, ws, (u32x4){a[0], b[0], a[1], b[1]});
      }
      STAMP(13);
      waitv<0>();
      if (ct_add(smem, C_Z, lane) == 7 && lane == 0) gst4c(WS_F3 + 4 * bx, ws, ep);
    }
    u32x4 xv[3], xn[3];
#pragma unroll
    for (int r = 0; r < 3; r++) xv[r] = gload16(x + (unsigned)((om0 + g2_row(r, w, lane)) * D_ + on0 + 8 * g2_pc(r, w, lane)));
    WAIT_L0();
    STAMP(14);
    BAR();
    STAMP(15);
    const unsigned asr = WS_H + ((mp * 8 + mh * 4 + w) * 224) * 256 + lane * 16;
    const unsigned bsr = WS_WDP + nt * (KP * 160) + w * 5120 + lane * 16;
    auto issA = [&](int kt) {
#pragma unroll
      for (int i = 0; i < 4; i++) glds16c(asr, ws + kt * 4096 + i * 1024, sm0 + (kt & 3) * G2_STAGE + w * 4096 + i * 1024);
    };
    auto issB = [&](int kt) {
#pragma unroll
      for (int i = 0; i < 5; i++) glds16c(bsr, ws + kt * 20480 + i * 1024, sm0 + (kt & 3) * G2_STAGE + G2_A + w * 5120 + i * 1024);
    };
    issB(0); issB(1); issB(2); issB(3);
    asm volatile("s_waitcnt vmcnt(20)" : "+v"(xv[0]), "+v"(xv[1]), "+v"(xv[2])::"memory");
#pragma unroll
    for (int r = 0; r < 3; r++) {
      const f32x2 ms = *(const f32x2*)(smem + L_MS + 8 * (64 * mh + g2_row(r, w, lane)));
      xn[r] = ln8(xv[r], ms[0], ms[1]);
    }
    asm volatile("" : "+v"(xn[0]), "+v"(xn[1]), "+v"(xn[2]));
    ct_wait(smem, C_GO, 1);
    STAMP(16);
    issA(0); issA(1); issA(2); issA(3);
    waitv<12>();
    STAMP(17);
    BAR();
    waitv<8>();
    BAR();
    issA(4); issB(4);
    waitv<13>();
    BAR();
    issA(5); issB(5);
    bool zd = false;
    const unsigned zsr = WS_Z + ((on0 + (lane >> 3)) * S_ + om0) * 2 + (lane & 7) * 16;
    auto issZ = [&]() {
#pragma unroll
      for (int i = 0; i < 3; i++) {
        const int q = (w + 4 * i) < 10 ? w + 4 * i : w;
        glds16c(zsr, ws + q * (8 * S_ * 2), sm0 + G2_ZS + q * 1024);
      }
    };
#pragma unroll 1
    for (int kt = 2; kt < G2_NKT - 4; kt++) {
      waitv<18>();
      BAR();
      issA(kt + 4); issB(kt + 4);
      if (!zd) {
        const unsigned f = *(volatile LDS_AS unsigned*)(smem + L_F3 + 4 * (lane & 31));
        if (__builtin_amdgcn_ballot_w64(f != ep) == 0) { issZ(); zd = true; }
      }
    }
    waitv<18>();
    BAR();
    waitv<9>();
    BAR();
    waitv<0>();
    STAMP(18);
    BAR();
    if (!zd) {
      if (!bad1 && poll_flags<1>(ws, WS_F3 + nt * 128 + 4 * (lane & 31), ep)) issZ();
      else ct_add(smem, C_BAD, lane);
      waitv<0>();
    }
    WAIT_L0();
    ct_add(smem, C_ZL, lane);
    ct_wait(smem, C_ZL, 4);
    STAMP(19);
#pragma unroll
    for (int r = 0; r < 3; r++) {
      const char* p = smem + G2_ZS + (8 * g2_pc(r, w, lane) + (r16 >> 2)) * 128 + 2 * (g2_row(r, w, lane) - r16) + 8 * (r16 & 3);
      const u32x2 zl = __builtin_bit_cast(u32x2, trread(p)), zh = __builtin_bit_cast(u32x2, trread(p + 512));
      sq[r] = (u32x4){sum2(xv[r][0], xn[r][0], zl[0]), sum2(xv[r][1], xn[r][1], zl[1]), sum2(xv[r][2], xn[r][2], zh[0]),
                      sum2(xv[r][3], xn[r][3], zh[1])};
    }
  } else {
    const int w = w8;
    __builtin_amdgcn_s_setprio(PRIO_M);
    if (w == 0) {
      *(unsigned*)(smem + L_CT + 4 * lane) = 0u;
      *(unsigned*)(smem + L_CT + 128 + 4 * lane) = 0u;
      unsigned long long pc;
      int dk;
      asm volatile("s_getpc_b64 %0\n.Lcw_here:\n\ts_mov_b32 %1, .Lcw_here-cw_all" : "=s"(pc), "=s"(dk));
      const unsigned long long c0 = pc - (long long)dk, c1 = (unsigned long long)(uintptr_t)&cw_code_end;
      const long long len = (long long)(c1 - c0);
      const unsigned off = (unsigned)(idx * 12 + lane) * 128;
      if (lane < 12 && (long long)off < len) gtouch(off, (const void*)c0, sm0 + L_DUMP);
    } else if (TDELAY) {
#pragma unroll 1
      for (int i = 0; i < TDELAY; i++) __builtin_amdgcn_s_sleep(1);
      if (w < 3) {
        const u16* src = w == 1 ? kin : vin;
        const unsigned o = ((kt0 + lane) * D_ + kd0) * 2;
        gtouch(o, src, sm0 + L_DUMP);
        gtouch(o + 156, src, sm0 + L_DUMP);
      } else {
        gtouch((cd0 * S_) * 2 + lane * 128, Kf, sm0 + L_DUMP);
        gtouch((cd2 * S_) * 2 + (lane & 31) * 128, Kf, sm0 + L_DUMP);
      }
    }
    const int wr = w >> 1, wc = w & 1;
    const int q = r16 >> 2, p = lane & 3;
    {
    int aptr[2];
#pragma unroll
    for (int ks = 0; ks < 2; ks++) {
      int row = wr * 64 + r16, chunk = ks * 4 + g;
      aptr[ks] = (row * 8 + (chunk ^ ((row >> 1) & 7))) * 16;
    }
    int taus[7];
#pragma unroll
    for (int tt = 0; tt < 7; tt++) {
      int j = (tt == 0) ? 0 : 1 + ((tt - 1 + 3 * wr) % 6);
      taus[tt] = wc ? 13 - j : j;
    }
    int bptr[7];
#pragma unroll
    for (int tt = 0; tt < 7; tt++)
      bptr[tt] = G1_A + (28 * (8 * g + q) + 2 * (taus[tt] ^ (g & 1)) + (p >> 1)) * 16 + 8 * (p & 1);
    const bool patch = (wc != odd) && (r16 == (odd ? 0 : 7));
    const int arot = wc * 4096;

    f32x4 acc[4][7], accg[2], accc[4];
#pragma unroll
    for (int i = 0; i < 4; i++)
#pragma unroll
      for (int j = 0; j < 7; j++) acc[i][j] = (f32x4){0.f, 0.f, 0.f, 0.f};
#pragma unroll
    for (int i = 0; i < 2; i++) accg[i] = (f32x4){0.f, 0.f, 0.f, 0.f};
#pragma unroll
    for (int i = 0; i < 4; i++) accc[i] = (f32x4){0.f, 0.f, 0.f, 0.f};
    const bf16x8 ones = {0x3F80, 0x3F80, 0x3F80, 0x3F80, 0x3F80, 0x3F80, 0x3F80, 0x3F80};
    int ap0[2] = {aptr[0] + arot, aptr[1] + arot};
    int ap1[2] = {aptr[0] + (arot ^ 4096), aptr[1] + (arot ^ 4096)};

    bf16x8 af0[4], bf0[7], af1[4], bf1_[7];
    auto rd = [&](int ks, bf16x8* af, bf16x8* bfr) {
#pragma unroll
      for (int tt = 0; tt < 7; tt++) {
        s16x4 lo = trread(smem + bptr[tt] + ks * 32 * 448);
        s16x4 hi = trread(smem + bptr[tt] + ks * 32 * 448 + 4 * 448);
        bfr[tt] = __builtin_shufflevector(lo, hi, 0, 1, 2, 3, 4, 5, 6, 7);
      }
      af[0] = *(const bf16x8*)(smem + ap0[ks]);
      af[1] = *(const bf16x8*)(smem + ap0[ks] + 2048);
      af[2] = *(const bf16x8*)(smem + ap1[ks]);
      af[3] = *(const bf16x8*)(smem + ap1[ks] + 2048);
    };
    auto mm = [&](bf16x8* af, bf16x8* bfr) {
      bf16x8 b0 = patch ? ones : bfr[0];
#pragma unroll
      for (int rt = 0; rt < 4; rt++)
#pragma unroll
        for (int tt = 1; tt < 7; tt++) acc[rt][tt] = MFMA16(bfr[tt], af[rt], acc[rt][tt]);
#pragma unroll
      for (int rt = 0; rt < 4; rt++) acc[rt][0] = MFMA16(b0, af[rt], acc[rt][0]);
#pragma unroll
      for (int j = 0; j < 2; j++) accg[j] = MFMA16(af[j], af[j], accg[j]);
      accc[0] = MFMA16(b0, ones, accc[0]);
#pragma unroll
      for (int j = 1; j < 4; j++) accc[j] = MFMA16(bfr[j], ones, accc[j]);
    };
    auto sched = [&]() {
#pragma unroll
      for (int i = 0; i < 9; i++) { SGB(0x8, 1); SGB(0x100, 1); SGB(0x8, 1); SGB(0x100, 1); SGB(0x8, 1); SGB(0x2, 1); }
      SGB(0x8, 7);
    };
    WAIT_L0();
    STAMP(1);
    BAR();
    STAMP(2);
    rd(0, af0, bf0);
    int buf = 0;
#pragma unroll 1
    for (int kt = 0; kt < NKT; kt++) {
      const int delta = (buf == 2) ? -2 * G1_STAGE : G1_STAGE;
      buf = (buf == 2) ? 0 : buf + 1;
      SB0();
      rd(1, af1, bf1_);
      mm(af0, bf0);
      sched();
      SB0();
      WAIT_L0();
      if (kt < 8) BAR();
      SB0();
#pragma unroll
      for (int tt = 0; tt < 7; tt++) bptr[tt] += delta;
      ap0[0] += delta; ap0[1] += delta; ap1[0] += delta; ap1[1] += delta;
      rd(0, af0, bf0);
      mm(af1, bf1_);
      sched();
      SB0();
    }
    WAIT_L0();
    STAMP(9);
    ct_add(smem, C_LOOP, lane);
    {
      const int e = r16 & 3;
#pragma unroll
      for (int j = 0; j < 2; j++) {
        const float lo = (e & 1) ? accg[j][1] : accg[j][0], hi = (e & 1) ? accg[j][3] : accg[j][2];
        if (q == g) *(float*)(smem + L_RQ + 4 * (wr * 64 + 16 * ((j + 2 * wc) & 3) + r16)) = (e & 2) ? hi : lo;
      }
      if (wc != odd && g == (odd ? 0 : 1)) {
#pragma unroll
        for (int rt = 0; rt < 4; rt++)
          *(float*)(smem + L_RS + 4 * (wr * 64 + 16 * ((rt + 2 * wc) & 3) + r16)) = odd ? acc[rt][0][0] : acc[rt][0][3];
      }
      if (r16 == 0) {
#pragma unroll
        for (int j = 0; j < 4; j++) *(f32x4*)(smem + L_CS + taus[j] * 64 + 16 * g) = accc[j];
      }
    }
    WAIT_L0();
    ct_add(smem, C_STAT, lane);
    ct_wait(smem, C_STAT, 4);
    STAMP(10);
    float mi[4], ir[4];
    f32x4 cs[7];
#pragma unroll
    for (int tt = 0; tt < 7; tt++) cs[tt] = *(const f32x4*)(smem + L_CS + taus[tt] * 64 + 16 * g);
#pragma unroll
    for (int rt = 0; rt < 4; rt++) {
      const int row = wr * 64 + 16 * ((rt + 2 * wc) & 3) + r16;
      const float rq = *(const float*)(smem + L_RQ + 4 * row), rs = *(const float*)(smem + L_RS + 4 * row);
      const float mean = rs * (1.0f / D_);
      const float var = fmaxf(rq * (1.0f / D_) - mean * mean, 0.f);
      const float mm_ = rbf(mean);
      const float sd_ = rbf(__builtin_amdgcn_sqrtf(rbf(rbf(var) + 1e-5f)));
      ir[rt] = __builtin_amdgcn_rcpf(sd_);
      mi[rt] = -mm_ * ir[rt];
      if (wc == 0 && g == 0) *(f32x2*)(smem + L_MS + 8 * row) = (f32x2){mm_, sd_};
    }
    const unsigned hb = WS_H + ((mp * 8 + wr * 4 + ((((g & 1) << 1) + (g >> 1) + 2 * wc) & 3)) * 224 + nw * 14) * 256 + r16 * 16;
    const bool zpad = (wc != odd) && ((g & 1) != odd);
#pragma unroll
    for (int tt = 0; tt < 7; tt++) {
      u32x2 o[4];
#pragma unroll
      for (int rt = 0; rt < 4; rt++) {
        const f32x4 v = acc[rt][tt] * ir[rt] + cs[tt] * mi[rt];
        o[rt] = (u32x2){pack2(v[0], v[1]), pack2(v[2], v[3])};
      }
      unsigned hh[2][2];
#pragma unroll
      for (int pr = 0; pr < 2; pr++)
#pragma unroll
        for (int i = 0; i < 2; i++) {
          const u32x2 s = __builtin_amdgcn_permlane32_swap(o[2 * pr][i], o[2 * pr + 1][i], false, false);
          hh[pr][i] = pack2(bflo(s[0]) * bflo(s[1]), bfhi(s[0]) * bfhi(s[1]));
          if (tt == 0 && zpad) hh[pr][i] = 0u;
        }
      const u32x2 a = __builtin_amdgcn_permlane16_swap(hh[0][0], hh[1][0], false, false);
      const u32x2 b = __builtin_amdgcn_permlane16_swap(hh[0][1], hh[1][1], false, false);
      gst16c(hb, ws + taus[tt] * 256, (u32x4){a[0], b[0], a[1], b[1]});
    }
    }
    STAMP(11);
    ct_wait(smem, C_BUILD, 4);
    const bool bad1 = ct_get(smem, C_BAD) != 0;
    {
      f32x4 a2[4];
      if (!bad1) {
        if (ce) cv_part<1>(smem + L_SP, lane, 2 * w, a2);
        else cv_part<0>(smem + L_SP, lane, 2 * w, a2);
        char* pw = smem + CV_P + 16384 + w * 1024 + lane * 16;
#pragma unroll
        for (int b = 0; b < 4; b++) *(f32x4*)(pw + b * 4096) = a2[b];
        WAIT_L0();
        ct_add(smem, C_P2, lane);
      }
      STAMP(12);
      waitv<0>();
      STAMP(13);
      if (ct_add(smem, C_H, lane) == 3 && lane == 0) gst4c(WS_F2 + 128 * mp + 4 * nw, ws, ep);
      if (!bad1) {
        const float dd = ld_D(Dv, cd2);
        ct_wait(smem, C_P2, 4);
        const char* pp = smem + CV_P + 16384 + w * 4096 + lane * 16;
        const f32x4 s = (*(const f32x4*)pp + *(const f32x4*)(pp + 1024)) + (*(const f32x4*)(pp + 2048) + *(const f32x4*)(pp + 3072));
        const int t0 = 256 * (ce ? 5 - w : (w < 2 ? 7 - w : 3 - w)) + 16 * r16 + 4 * g;
        gst8c(WS_Z + (cd2 * S_ + t0) * 2, ws, cv_z(s, smem + L_SP + CV_UO + 2 * (2044 - t0), dd));
        waitv<0>();
        if (ct_add(smem, C_Z, lane) == 7 && lane == 0) gst4c(WS_F3 + 4 * bx, ws, ep);
      }
    }
    WAIT_L0();
    STAMP(14);
    BAR();
    STAMP(15);
    if (w == 0) {
      const bool ok2 = poll_flags<1>(ws, WS_F2 + 128 * mp + 4 * (lane & 15), ep);
      if (!ok2) ct_add(smem, C_BAD, lane);
      ct_add(smem, C_GO, lane);
    }
    STAMP(16);
    const int kh = w >> 1, wq = w & 1;
    const int aoff = 2 * wq * 4096 + (kh * 8 + g) * 256 + r16 * 16;
    const int boff = G2_A + (64 * kh + 16 * (g >> 1) + 4 * (g & 1) + q) * 160 + p * 8;
    f32x4 acc[2][5];
#pragma unroll
    for (int i = 0; i < 2; i++)
#pragma unroll
      for (int j = 0; j < 5; j++) acc[i][j] = (f32x4){0.f, 0.f, 0.f, 0.f};
    bf16x8 af0[4], bf0[10], af1[4], bf1_[10];
    auto rd = [&](int kt, bf16x8* af, bf16x8* bfr) {
      const char* base = smem + (kt & 3) * G2_STAGE;
#pragma unroll
      for (int s = 0; s < 2; s++) {
#pragma unroll
        for (int rt = 0; rt < 2; rt++) af[2 * s + rt] = *(const bf16x8*)(base + aoff + rt * 4096 + s * 1024);
#pragma unroll
        for (int tt = 0; tt < 5; tt++) {
          s16x4 lo = trread(base + boff + s * 5120 + tt * 32);
          s16x4 hi = trread(base + boff + s * 5120 + tt * 32 + 1280);
          bfr[5 * s + tt] = __builtin_shufflevector(lo, hi, 0, 1, 2, 3, 4, 5, 6, 7);
        }
      }
    };
    auto mm = [&](bf16x8* af, bf16x8* bfr) {
#pragma unroll
      for (int s = 0; s < 2; s++)
#pragma unroll
        for (int tt = 0; tt < 5; tt++)
#pragma unroll
          for (int rt = 0; rt < 2; rt++) acc[rt][tt] = MFMA16(bfr[5 * s + tt], af[2 * s + rt], acc[rt][tt]);
    };
    auto sched = [&]() {
#pragma unroll
      for (int i = 0; i < 12; i++) { SGB(0x100, 2); SGB(0x8, 1); }
      SGB(0x8, 8);
    };
    const unsigned f3o = WS_F3 + nt * 128 + 4 * (lane & 31);
    BAR();
    STAMP(17);
    rd(0, af0, bf0);
    WAIT_L0();
#pragma unroll 1
    for (int kt = 0; kt < G2_NKT - 2; kt += 2) {
      BAR();
      SB0();
      if (w == 0) glds4c(f3o, ws, sm0 + L_F3);
      rd(kt + 1, af1, bf1_);
      mm(af0, bf0);
      sched();
      SB0();
      WAIT_L0();
      BAR();
      SB0();
      rd(kt + 2, af0, bf0);
      mm(af1, bf1_);
      sched();
      SB0();
      WAIT_L0();
    }
    BAR();
    STAMP(18);
    SB0();
    rd(G2_NKT - 1, af1, bf1_);
    mm(af0, bf0);
    sched();
    SB0();
    mm(af1, bf1_);
    float* red = (float*)(smem + 2 * G2_STAGE);
#pragma unroll
    for (int rt = 0; rt < 2; rt++)
#pragma unroll
      for (int tt = 0; tt < 5; tt++)
        *(f32x4*)(red + (kh * 64 + wq * 32 + rt * 16 + r16) * 84 + tt * 16 + 4 * g) = acc[rt][tt];
    waitv<0>();
  }
  WAIT_L0();
  STAMP(20);
  BAR();
  STAMP(21);
  if (__builtin_expect(ct_get(smem, C_BAD) != 0, 0)) {
    slow_tile(smem, x, kin, vin, sw, Kf, Dv, wg, wu, wd, out, om0, on0);
    STAMP(25);
  } else if (w8 >= 4) {
    const int w = w8 - 4;
#pragma unroll
    for (int r = 0; r < 3; r++) {
      const float* rp = (const float*)(smem + 2 * G2_STAGE) + g2_row(r, w, lane) * 84 + 8 * g2_pc(r, w, lane);
      const f32x4 b0 = *(const f32x4*)rp + *(const f32x4*)(rp + 64 * 84);
      const f32x4 b1 = *(const f32x4*)(rp + 4) + *(const f32x4*)(rp + 64 * 84 + 4);
      const u32x4 bq = {pack2(b0[0], b0[1]), pack2(b0[2], b0[3]), pack2(b1[0], b1[1]), pack2(b1[2], b1[3])};
      u32x4 o;
#pragma unroll
      for (int i = 0; i < 4; i++) o[i] = pack2(bflo(sq[r][i]) + bflo(bq[i]), bfhi(sq[r][i]) + bfhi(bq[i]));
      if (r < 2 || lane < 32) *(u32x4*)(out + (om0 + g2_row(r, w, lane)) * D_ + on0 + 8 * g2_pc(r, w, lane)) = o;
    }
  }
  STAMP(22);
  STAMP(23); STAMPR(31);
  STAMP_OUT;
}

extern "C" void kernel_launch(void* const* d_in, const int* in_sizes, int n_in, void* d_out, int out_size,
                              void* d_ws, size_t ws_size, hipStream_t stream) {
  const u16* x = (const u16*)d_in[0];
  const u16* k = (const u16*)d_in[1];
  const u16* v = (const u16*)d_in[2];
  const u16* sw = (const u16*)d_in[3];
  const u16* Kf = (const u16*)d_in[4];
  const u16* Dv = (const u16*)d_in[5];
  const u16* wg = (const u16*)d_in[7];
  const u16* wu = (const u16*)d_in[8];
  const u16* wd = (const u16*)d_in[9];
  (void)hipFuncSetAttribute((const void*)cw_all, hipFuncAttributeMaxDynamicSharedMemorySize, K_LDS);
  cw_all<<<256, 512, K_LDS, stream>>>(x, wg, wu, (char*)d_ws, Kf, wd, k, v, sw, Dv, (u16*)d_out);
}
```

```cpp
#include <hip/hip_runtime.h>
#include <hip/hip_bf16.h>
#include <stdint.h>

typedef unsigned short u16;
typedef __attribute__((ext_vector_type(8))) short bf16x8;
typedef __attribute__((ext_vector_type(4))) short s16x4;
typedef __attribute__((ext_vector_type(4))) float f32x4;
typedef __attribute__((ext_vector_type(2))) float f32x2;
typedef __attribute__((ext_vector_type(4))) unsigned u32x4;
typedef __attribute__((ext_vector_type(2))) unsigned u32x2;
typedef __attribute__((ext_vector_type(2))) __bf16 bf16x2_t;
#define LDS_AS __attribute__((address_space(3)))

#define ABL 0
constexpr int S_ = 2048, D_ = 640, F_ = 1728;

__device__ __forceinline__ float bflo(unsigned v) { return __uint_as_float(v << 16); }
__device__ __forceinline__ float bfhi(unsigned v) { return __uint_as_float(v & 0xffff0000u); }
__device__ __forceinline__ unsigned pack2(float a, float b) {
  f32x2 v = {a, b};
  bf16x2_t r = __builtin_convertvector(v, bf16x2_t);
  return __builtin_bit_cast(unsigned, r);
}
__device__ __forceinline__ float rbf(float a) { __bf16 r = (__bf16)a; return (float)r; }
__device__ __forceinline__ float bf1(u16 v) { return __uint_as_float((unsigned)v << 16); }

__device__ __forceinline__ void glds16(unsigned voff, const void* sbase, unsigned lds_addr) {
  asm volatile("s_mov_b32 m0, %2\n\ts_nop 0\n\tglobal_load_lds_dwordx4 %0, %1" ::"v"(voff), "s"(sbase), "s"(lds_addr) : "memory");
}
__device__ __forceinline__ void glds16c(unsigned voff, const void* sbase, unsigned lds_addr) {
  asm volatile("s_mov_b32 m0, %2\n\ts_nop 0\n\tglobal_load_lds_dwordx4 %0, %1 sc1" ::"v"(voff), "s"(sbase), "s"(lds_addr) : "memory");
}
__device__ __forceinline__ void glds16v(const void* vptr, unsigned lds_addr) {
  asm volatile("s_mov_b32 m0, %1\n\ts_nop 0\n\tglobal_load_lds_dwordx4 %0, off" ::"v"(vptr), "s"(lds_addr) : "memory");
}
#define NTS 1
#define NTG 0
__device__ __forceinline__ void glds16n(unsigned voff, const void* sbase, unsigned lds_addr) {
  asm volatile("s_mov_b32 m0, %2\n\ts_nop 0\n\tglobal_load_lds_dwordx4 %0, %1 nt" ::"v"(voff), "s"(sbase), "s"(lds_addr) : "memory");
}
__device__ __forceinline__ void glds16vn(const void* vptr, unsigned lds_addr) {
  asm volatile("s_mov_b32 m0, %1\n\ts_nop 0\n\tglobal_load_lds_dwordx4 %0, off nt" ::"v"(vptr), "s"(lds_addr) : "memory");
}
__device__ __forceinline__ u32x4 gload16n(const void* p) {
  u32x4 r;
  asm volatile("global_load_dwordx4 %0, %1, off nt" : "=v"(r) : "v"(p) : "memory");
  return r;
}
__device__ __forceinline__ u32x2 gload8n(const void* p) {
  u32x2 r;
  asm volatile("global_load_dwordx2 %0, %1, off nt" : "=v"(r) : "v"(p) : "memory");
  return r;
}
__device__ __forceinline__ void gtouch(unsigned voff, const void* sbase, unsigned lds_addr) {
  asm volatile("s_mov_b32 m0, %2\n\ts_nop 0\n\tglobal_load_lds_dword %0, %1" ::"v"(voff), "s"(sbase), "s"(lds_addr) : "memory");
}
__device__ __forceinline__ void gtouchc(unsigned voff, const void* sbase, unsigned lds_addr) {
  asm volatile("s_mov_b32 m0, %2\n\ts_nop 0\n\tglobal_load_lds_dword %0, %1 sc1" ::"v"(voff), "s"(sbase), "s"(lds_addr) : "memory");
}
template <int OFF> __device__ __forceinline__ bf16x8 gl16p(unsigned voff, const void* sbase) {
  bf16x8 r;
  asm volatile("global_load_dwordx4 %0, %1, %2 offset:%3" : "=v"(r) : "v"(voff), "s"(sbase), "n"(OFF) : "memory");
  return r;
}
template <int OFF> __device__ __forceinline__ bf16x8 gl16c(unsigned voff, const void* sbase) {
  bf16x8 r;
  asm volatile("global_load_dwordx4 %0, %1, %2 offset:%3 sc1" : "=v"(r) : "v"(voff), "s"(sbase), "n"(OFF) : "memory");
  return r;
}
__device__ __forceinline__ u32x4 gload16(const void* p) {
  u32x4 r;
  asm volatile("global_load_dwordx4 %0, %1, off" : "=v"(r) : "v"(p) : "memory");
  return r;
}
__device__ __forceinline__ u32x2 gload8(const void* p) {
  u32x2 r;
  asm volatile("global_load_dwordx2 %0, %1, off" : "=v"(r) : "v"(p) : "memory");
  return r;
}
__device__ __forceinline__ void gst16c(unsigned voff, void* sbase, const u32x4 v) {
  asm volatile("global_store_dwordx4 %0, %1, %2 sc1\n\ts_nop 1" ::"v"(voff), "v"(v), "s"(sbase) : "memory");
}
#define NTST 0
template <int NT> __device__ __forceinline__ void gst16t(unsigned voff, void* sbase, const u32x4 v) {
  if (NT) asm volatile("global_store_dwordx4 %0, %1, %2 sc1 nt\n\ts_nop 1" ::"v"(voff), "v"(v), "s"(sbase) : "memory");
  else asm volatile("global_store_dwordx4 %0, %1, %2 sc1\n\ts_nop 1" ::"v"(voff), "v"(v), "s"(sbase) : "memory");
}
__device__ __forceinline__ void gst8c(unsigned voff, void* sbase, const u32x2 v) {
  asm volatile("global_store_dwordx2 %0, %1, %2 sc1" ::"v"(voff), "v"(v), "s"(sbase) : "memory");
}
__device__ __forceinline__ void gst4c(unsigned voff, void* sbase, const unsigned v) {
  asm volatile("global_store_dword %0, %1, %2 sc1" ::"v"(voff), "v"(v), "s"(sbase) : "memory");
}
__device__ __forceinline__ unsigned lds_off(const void* p) { return (unsigned)(uintptr_t)(const LDS_AS char*)p; }
__device__ __forceinline__ s16x4 trread(const char* lds) {
  return __builtin_amdgcn_ds_read_tr16_b64_v4i16((LDS_AS s16x4*)lds);
}
#define WAIT_V(n) asm volatile("s_waitcnt vmcnt(" #n ")" ::: "memory")
#define WAIT_L0() asm volatile("s_waitcnt lgkmcnt(0)" ::: "memory")

#define STAMPS 0
#define SPIN_MAX 1500
#define STAMP_DECL
#define STAMP_OUT
#define STAMP(i)
#define STAMPR(i)
#define SGB(m, n) __builtin_amdgcn_sched_group_barrier(m, n, 0)
#define SB0() __builtin_amdgcn_sched_barrier(0)
#define BAR() asm volatile("s_barrier" ::: "memory")
#define MFMA16(a, b, c) __builtin_amdgcn_mfma_f32_16x16x32_bf16(a, b, c, 0, 0, 0)
template <int N> __device__ __forceinline__ void waitv() { asm volatile("s_waitcnt vmcnt(%0)" ::"n"(N) : "memory"); }
extern "C" __device__ unsigned long long cw_dispatch_id() __asm("llvm.amdgcn.dispatch.id");

constexpr int KP = 1792;
constexpr unsigned WS_WDP = 0;
#define FS1 4
#define FS2 128
#define FS3 128
#define FSP 4
constexpr unsigned WS_F1 = 3u << 20, WS_F2 = WS_F1 + 65536, WS_F3 = WS_F1 + 2 * 65536;
constexpr unsigned WS_FP = WS_F1 + 3 * 65536;
constexpr unsigned WS_G = WS_F1 + 5 * 65536;
constexpr unsigned F2M = FS2 == 4 ? 128 : 16 * FS2;
constexpr unsigned WS_Z = 4u << 20;
constexpr unsigned WS_H = 8u << 20;
constexpr unsigned WS_KV = 16u << 20;
constexpr int G1_A = 16384, G1_B = 28672, G1_STAGE = G1_A + G1_B;
constexpr int L_SP = 3 * G1_STAGE;
constexpr int KVS_V = 11264, KVS_W = 21504;
constexpr int CV_CS = 4160;
constexpr int CV_UO = 4 * CV_CS;
constexpr int CV_R = CV_UO + 4576;
constexpr int CV_R0 = G1_STAGE;
constexpr int CV_P = 2 * G1_STAGE;
constexpr int G2_ZS = CV_P + 32768;
constexpr int L_ST = 160000;
constexpr int L_RQ = L_ST, L_RS = L_ST + 512, L_CS = L_ST + 1024, L_MS = L_ST + 1920;
constexpr int L_CT = L_ST + 2944;
constexpr int L_DUMP = L_CT + 256;
constexpr int K_LDS = L_DUMP + 256;
enum { C_STAT, C_LOOP, C_BUILD, C_PA, C_Z, C_H, C_BAD, C_ZL, C_S9, C_KV, C_RD, C_F1, C_F1K, C_CV, C_BAD1, C_F3K, C_B2, C_G2, C_BR };

__device__ __forceinline__ unsigned ct_add(char* smem, const int c, const int lane) {
  unsigned r = 0;
  if (lane == 0) r = __hip_atomic_fetch_add((LDS_AS unsigned*)(smem + L_CT + 4 * c), 1u, __ATOMIC_RELAXED, __HIP_MEMORY_SCOPE_WORKGROUP);
  return __builtin_amdgcn_readfirstlane(r);
}
__device__ __forceinline__ unsigned ct_get(char* smem, const int c) {
  return __builtin_amdgcn_readfirstlane(*(volatile LDS_AS unsigned*)(smem + L_CT + 4 * c));
}
__device__ __forceinline__ void ct_wait(char* smem, const int c, const unsigned n) {
  while (ct_get(smem, c) < n) __builtin_amdgcn_s_sleep(1);
  asm volatile("" ::: "memory");
}
#define FPD 1
#define PSLEEP 2
struct CvK { u32x2 lo; u32x4 h0, h1; };
__device__ __forceinline__ void cv_ldk(CvK& in, const int d, const int j, const u16* __restrict__ Kf) {
  const u16* Kd = Kf + d * S_ + 16 * j;
  in.h0 = gload16n(Kd);
  in.h1 = gload16n(Kd + 8);
  in.lo = gload8n(Kd - (j > 0 ? 4 : 0));
}
__device__ __forceinline__ void cv_b1(const unsigned l0, const unsigned l1, const u32x4 hi, char* p) {
  const unsigned v2 = __builtin_amdgcn_alignbit(l1, l0, 16), v3 = __builtin_amdgcn_alignbit(hi[0], l1, 16);
  const unsigned v4 = __builtin_amdgcn_alignbit(hi[1], hi[0], 16), v5 = __builtin_amdgcn_alignbit(hi[2], hi[1], 16);
  const unsigned v6 = __builtin_amdgcn_alignbit(hi[3], hi[2], 16);
  *(u32x4*)(p + 3 * CV_CS) = hi;
  *(u32x4*)(p) = (u32x4){v2, v3, v4, v5};
  *(u32x4*)(p + CV_CS) = (u32x4){l1, hi[0], hi[1], hi[2]};
  *(u32x4*)(p + 2 * CV_CS) = (u32x4){v3, v4, v5, v6};
}
__device__ __forceinline__ void cv_build(const CvK& in, char* reg, const int j) {
  cv_b1(j > 0 ? in.lo[0] : 0u, j > 0 ? in.lo[1] : 0u, in.h0, reg + 32 * j + 32);
  cv_b1(in.h0[2], in.h0[3], in.h1, reg + 32 * j + 48);
  if (j < 38) {
    const int o = j < 8 ? CV_CS * (j >> 1) + 16 * (j & 1) : CV_UO + 4096 + 16 * (j - 8);
    *(u32x4*)(reg + o) = (u32x4){0, 0, 0, 0};
  }
}
__device__ __forceinline__ bf16x8 cv_ra(const char* p) {
  const s16x4 lo = *(const s16x4*)p, hi = *(const s16x4*)(p + 8);
  return __builtin_shufflevector(lo, hi, 0, 1, 2, 3, 4, 5, 6, 7);
}
__device__ __forceinline__ void cv_half(const char* reg, const int lane, const int s0, f32x4* acc, const unsigned fpa,
                                        char* ws, const unsigned ep) {
  const int c = lane & 15, g = lane >> 4;
#pragma unroll
  for (int n = 0; n < 8; n++) acc[n] = (f32x4){0.f, 0.f, 0.f, 0.f};
  const char* ab = reg + CV_CS * (c & 3) + 64 * s0 + 16 * g + 8 * (c >> 2) + 8;
  const char* bb = reg + CV_UO + 4064 - 32 * c + 64 * s0 + 16 * g - 512 * 7;
  bf16x8 G[2][8], A[2][8];
  SB0();
  A[0][0] = cv_ra(ab);
#pragma unroll
  for (int nu = 7; nu >= 0; nu--) G[0][nu] = *(const bf16x8*)(bb + 512 * (7 - nu));
  A[0][1] = cv_ra(ab + 512);
#pragma unroll
  for (int s = 0; s < 4; s++) {
    const int cu = s & 1, nx = cu ^ 1;
#pragma unroll
    for (int p = 0; p < 8; p++) {
      SB0();
      if (p + 2 < 8) A[cu][p + 2] = cv_ra(ab + 64 * s + 512 * (p + 2));
      else if (s < 3) A[nx][p - 6] = cv_ra(ab + 64 * (s + 1) + 512 * (p - 6));
      if (s < 3 && p > 0) G[nx][8 - p] = *(const bf16x8*)(bb + 64 * (s + 1) + 512 * (p - 1));
      if (s > 0 && p == 0) G[cu][0] = *(const bf16x8*)(bb + 64 * s + 512 * 7);
      SB0();
#pragma unroll
      for (int n = 7; n >= p; n--) acc[n] = MFMA16(A[cu][p], G[cu][n - p], acc[n]);
    }
    SB0();
    if (s == FPD - 1 && fpa) {
      waitv<0>();
      if (lane == 0) gst4c(fpa, ws, ep);
    }
  }
}
template <int E>
__device__ __forceinline__ void cv_part(const char* reg, const int lane, const int sl, f32x4* acc) {
  const int c = lane & 15, g = lane >> 4;
  const char* ab = reg + CV_CS * (c & 3) + 64 * sl + 16 * g + 8 * (c >> 2) + 8;
  const char* bb = reg + CV_UO + 4064 - 32 * c + 64 * sl + 16 * g - 512 * 7;
  constexpr int NT = E ? 6 : 8;
  bf16x8 G[2][NT], A[2][NT];
#pragma unroll
  for (int s = 0; s < 2; s++)
#pragma unroll
    for (int p = 0; p < NT; p++) {
      A[s][p] = cv_ra(ab + 64 * s + 512 * p);
      G[s][NT - 1 - p] = *(const bf16x8*)(bb + 64 * s + 512 * (8 - NT + p));
    }
#pragma unroll
  for (int n = 0; n < 4; n++) acc[n] = (f32x4){0.f, 0.f, 0.f, 0.f};
#pragma unroll
  for (int s = 0; s < 2; s++)
#pragma unroll
    for (int p = 0; p < NT; p++)
#pragma unroll
      for (int b = 0; b < 4; b++) {
        const int n = E ? 5 - b : (b < 2 ? 7 - b : 3 - b);
        if (n >= p) acc[b] = MFMA16(A[s][p], G[s][n - p], acc[b]);
      }
}
__device__ __forceinline__ u32x2 cv_z(const f32x4 s, const char* kvp, const float dd) {
  const u32x2 kv = *(const u32x2*)kvp;
  const unsigned y0 = pack2(s[0], s[1]), y1 = pack2(s[2], s[3]);
  const unsigned k0 = pack2(bfhi(kv[1]) * dd, bflo(kv[1]) * dd), k1 = pack2(bfhi(kv[0]) * dd, bflo(kv[0]) * dd);
  return (u32x2){pack2(bflo(y0) + bflo(k0), bfhi(y0) + bfhi(k0)), pack2(bflo(y1) + bflo(k1), bfhi(y1) + bfhi(k1))};
}
__device__ __forceinline__ float rb16(const float a) { return __uint_as_float(pack2(0.f, a)); }
__device__ __forceinline__ u32x4 kv8(const u32x2 kp, const u32x2 kl, const u32x2 kh, const u32x2 vl, const u32x2 vh, const u32x2 wv) {
  const float w0 = bflo(wv[0]), w1 = bfhi(wv[0]), w2 = bflo(wv[1]), w3 = bfhi(wv[1]);
  const float kk[11] = {bfhi(kp[0]), bflo(kp[1]), bfhi(kp[1]), bflo(kl[0]), bfhi(kl[0]), bflo(kl[1]),
                        bfhi(kl[1]), bflo(kh[0]), bfhi(kh[0]), bflo(kh[1]), bfhi(kh[1])};
  const float vf[8] = {bflo(vl[0]), bfhi(vl[0]), bflo(vl[1]), bfhi(vl[1]), bflo(vh[0]), bfhi(vh[0]), bflo(vh[1]), bfhi(vh[1])};
  float a[8];
#define KV_FMA(d, x, y, z) asm("v_fma_f32 %0, %1, %2, %3" : "=v"(d) : "v"(x), "v"(y), "v"(z))
#define KV_MUL(d, x, y) asm("v_mul_f32 %0, %1, %2" : "=v"(d) : "v"(x), "v"(y))
#pragma unroll
  for (int e = 0; e < 8; e++) KV_MUL(a[e], kk[e], w3);
#pragma unroll
  for (int e = 0; e < 8; e++) KV_FMA(a[e], kk[e + 1], w2, a[e]);
#pragma unroll
  for (int e = 0; e < 8; e++) KV_FMA(a[e], kk[e + 2], w1, a[e]);
#pragma unroll
  for (int e = 0; e < 8; e++) KV_FMA(a[e], kk[e + 3], w0, a[e]);
#pragma unroll
  for (int e = 0; e < 8; e++) a[e] = rb16(a[e]);
#pragma unroll
  for (int e = 0; e < 8; e++) KV_MUL(a[e], a[e], vf[e]);
  u32x4 kvr;
#pragma unroll
  for (int m = 0; m < 4; m++) kvr[3 - m] = pack2(a[2 * m + 1], a[2 * m]);
  return kvr;
}
__device__ __forceinline__ u32x4 ln8(const u32x4 xv, const float m, float sd) {
  asm volatile("" : "+v"(sd));
  const float r = __builtin_amdgcn_rcpf(sd);
  u32x4 o;
#pragma unroll
  for (int i = 0; i < 4; i++) {
    const unsigned a = pack2(bflo(xv[i]) - m, bfhi(xv[i]) - m);
    const float a0 = bflo(a), a1 = bfhi(a);
    float q0 = a0 * r, q1 = a1 * r;
    q0 = fmaf(fmaf(-q0, sd, a0), r, q0);
    q1 = fmaf(fmaf(-q1, sd, a1), r, q1);
    o[i] = pack2(q0, q1);
  }
  return o;
}
__device__ __forceinline__ unsigned sum2(const unsigned xv, const unsigned xn, const unsigned z) {
  const unsigned a = pack2(bflo(xn) * bflo(z), bfhi(xn) * bfhi(z));
  return pack2(bflo(xv) + bflo(a), bfhi(xv) + bfhi(a));
}
__device__ __forceinline__ int g2_row(const int r, const int w, const int lane) {
  const int g = lane >> 4;
  return 16 * (r < 2 ? 2 * r + (g >> 1) : 2 * (w & 1) + (g & 1)) + (lane & 15);
}
__device__ __forceinline__ int g2_pc(const int r, const int w, const int lane) { return r < 2 ? 2 * w + ((lane >> 4) & 1) : 8 + (w >> 1); }
template <int NW, int NS = SPIN_MAX>
__device__ __forceinline__ bool poll_flags(const char* ws, const unsigned voff, const unsigned ep) {
  bool ok = false;
#pragma unroll 1
  for (int spin = 0; spin < NS; spin++) {
    bool b;
    if (NW == 4) {
      u32x4 r;
      asm volatile("global_load_dwordx4 %0, %1, %2 sc1\n\ts_waitcnt vmcnt(0)" : "=v"(r) : "v"(voff), "s"(ws) : "memory");
      b = (r[0] != ep) | (r[1] != ep) | (r[2] != ep) | (r[3] != ep);
    } else {
      unsigned r;
      asm volatile("global_load_dword %0, %1, %2 sc1\n\ts_waitcnt vmcnt(0)" : "=v"(r) : "v"(voff), "s"(ws) : "memory");
      b = r != ep;
    }
    if (__builtin_amdgcn_ballot_w64(b) == 0) { ok = true; break; }
    __builtin_amdgcn_s_sleep(PSLEEP);
  }
  return ok;
}
__device__ __forceinline__ bool sflags32(const char* p, const unsigned ep) {
  unsigned r;
  asm volatile(
      "s_load_dword s60, %1, 0 glc\n\t"
      "s_load_dword s61, %1, 128 glc\n\t"
      "s_load_dword s62, %1, 256 glc\n\t"
      "s_load_dword s63, %1, 384 glc\n\t"
      "s_load_dword s64, %1, 512 glc\n\t"
      "s_load_dword s65, %1, 640 glc\n\t"
      "s_load_dword s66, %1, 768 glc\n\t"
      "s_load_dword s67, %1, 896 glc\n\t"
      "s_load_dword s68, %1, 1024 glc\n\t"
      "s_load_dword s69, %1, 1152 glc\n\t"
      "s_load_dword s70, %1, 1280 glc\n\t"
      "s_load_dword s71, %1, 1408 glc\n\t"
      "s_load_dword s72, %1, 1536 glc\n\t"
      "s_load_dword s73, %1, 1664 glc\n\t"
      "s_load_dword s74, %1, 1792 glc\n\t"
      "s_load_dword s75, %1, 1920 glc\n\t"
      "s_load_dword s76, %1, 2048 glc\n\t"
      "s_load_dword s77, %1, 2176 glc\n\t"
      "s_load_dword s78, %1, 2304 glc\n\t"
      "s_load_dword s79, %1, 2432 glc\n\t"
      "s_load_dword s80, %1, 2560 glc\n\t"
      "s_load_dword s81, %1, 2688 glc\n\t"
      "s_load_dword s82, %1, 2816 glc\n\t"
      "s_load_dword s83, %1, 2944 glc\n\t"
      "s_load_dword s84, %1, 3072 glc\n\t"
      "s_load_dword s85, %1, 3200 glc\n\t"
      "s_load_dword s86, %1, 3328 glc\n\t"
      "s_load_dword s87, %1, 3456 glc\n\t"
      "s_load_dword s88, %1, 3584 glc\n\t"
      "s_load_dword s89, %1, 3712 glc\n\t"
      "s_load_dword s90, %1, 3840 glc\n\t"
      "s_load_dword s91, %1, 3968 glc\n\t"
      "s_waitcnt lgkmcnt(0)\n\ts_xor_b32 %0, s60, %2\n\t"
      "s_xor_b32 s61, s61, %2\n\ts_or_b32 %0, %0, s61\n\t"
      "s_xor_b32 s62, s62, %2\n\ts_or_b32 %0, %0, s62\n\t"
      "s_xor_b32 s63, s63, %2\n\ts_or_b32 %0, %0, s63\n\t"
      "s_xor_b32 s64, s64, %2\n\ts_or_b32 %0, %0, s64\n\t"
      "s_xor_b32 s65, s65, %2\n\ts_or_b32 %0, %0, s65\n\t"
      "s_xor_b32 s66, s66, %2\n\ts_or_b32 %0, %0, s66\n\t"
      "s_xor_b32 s67, s67, %2\n\ts_or_b32 %0, %0, s67\n\t"
      "s_xor_b32 s68, s68, %2\n\ts_or_b32 %0, %0, s68\n\t"
      "s_xor_b32 s69, s69, %2\n\ts_or_b32 %0, %0, s69\n\t"
      "s_xor_b32 s70, s70, %2\n\ts_or_b32 %0, %0, s70\n\t"
      "s_xor_b32 s71, s71, %2\n\ts_or_b32 %0, %0, s71\n\t"
      "s_xor_b32 s72, s72, %2\n\ts_or_b32 %0, %0, s72\n\t"
      "s_xor_b32 s73, s73, %2\n\ts_or_b32 %0, %0, s73\n\t"
      "s_xor_b32 s74, s74, %2\n\ts_or_b32 %0, %0, s74\n\t"
      "s_xor_b32 s75, s75, %2\n\ts_or_b32 %0, %0, s75\n\t"
      "s_xor_b32 s76, s76, %2\n\ts_or_b32 %0, %0, s76\n\t"
      "s_xor_b32 s77, s77, %2\n\ts_or_b32 %0, %0, s77\n\t"
      "s_xor_b32 s78, s78, %2\n\ts_or_b32 %0, %0, s78\n\t"
      "s_xor_b32 s79, s79, %2\n\ts_or_b32 %0, %0, s79\n\t"
      "s_xor_b32 s80, s80, %2\n\ts_or_b32 %0, %0, s80\n\t"
      "s_xor_b32 s81, s81, %2\n\ts_or_b32 %0, %0, s81\n\t"
      "s_xor_b32 s82, s82, %2\n\ts_or_b32 %0, %0, s82\n\t"
      "s_xor_b32 s83, s83, %2\n\ts_or_b32 %0, %0, s83\n\t"
      "s_xor_b32 s84, s84, %2\n\ts_or_b32 %0, %0, s84\n\t"
      "s_xor_b32 s85, s85, %2\n\ts_or_b32 %0, %0, s85\n\t"
      "s_xor_b32 s86, s86, %2\n\ts_or_b32 %0, %0, s86\n\t"
      "s_xor_b32 s87, s87, %2\n\ts_or_b32 %0, %0, s87\n\t"
      "s_xor_b32 s88, s88, %2\n\ts_or_b32 %0, %0, s88\n\t"
      "s_xor_b32 s89, s89, %2\n\ts_or_b32 %0, %0, s89\n\t"
      "s_xor_b32 s90, s90, %2\n\ts_or_b32 %0, %0, s90\n\t"
      "s_xor_b32 s91, s91, %2\n\ts_or_b32 %0, %0, s91\n\t"
      : "=&s"(r) : "s"(p), "s"(ep) : "s60", "s61", "s62", "s63", "s64", "s65", "s66", "s67", "s68", "s69", "s70", "s71", "s72", "s73", "s74", "s75", "s76", "s77", "s78", "s79", "s80", "s81", "s82", "s83", "s84", "s85", "s86", "s87", "s88", "s89", "s90", "s91", "scc", "memory");
  return r == 0;
}
__device__ __forceinline__ bool sflags4(const char* p, const unsigned ep) {
  unsigned r;
  asm volatile(
      "s_load_dword s60, %1, 0 glc\n\t"
      "s_load_dword s61, %1, 128 glc\n\t"
      "s_load_dword s62, %1, 256 glc\n\t"
      "s_load_dword s63, %1, 384 glc\n\t"
      "s_waitcnt lgkmcnt(0)\n\ts_xor_b32 %0, s60, %2\n\t"
      "s_xor_b32 s61, s61, %2\n\ts_or_b32 %0, %0, s61\n\t"
      "s_xor_b32 s62, s62, %2\n\ts_or_b32 %0, %0, s62\n\t"
      "s_xor_b32 s63, s63, %2\n\ts_or_b32 %0, %0, s63\n\t"
      : "=&s"(r) : "s"(p), "s"(ep) : "s60", "s61", "s62", "s63", "scc", "memory");
  return r == 0;
}
#define SYNC() do { asm volatile("s_waitcnt vmcnt(0) lgkmcnt(0)\n\ts_barrier" ::: "memory"); } while (0)
__device__ __forceinline__ void slow_tile(char* smem, const u16* __restrict__ x, const u16* __restrict__ kin,
                                          const u16* __restrict__ vin, const u16* __restrict__ sw, const u16* __restrict__ Kf,
                                          const u16* __restrict__ Dv, const u16* __restrict__ wg, const u16* __restrict__ wu,
                                          const u16* __restrict__ wd, u16* __restrict__ out, const int m0, const int n0) {
  int tid = threadIdx.x;
  asm volatile("" : "+v"(tid));
  const int lane = tid & 63, w8 = tid >> 6;
  u16* xn = (u16*)smem;
  u16* H = (u16*)(smem + 81920);
  float* kvs = (float*)(smem + 90112);
  float* red = (float*)(smem + 98304);
  u16* Z = (u16*)(smem + 100352);
  SYNC();
#pragma unroll 1
  for (int r = w8 * 8; r < w8 * 8 + 8; r++) {
    const u16* xr = x + (m0 + r) * D_;
    float s = 0.f;
    for (int j = lane; j < D_; j += 64) s += bf1(xr[j]);
    for (int o = 32; o >= 1; o >>= 1) s += __shfl_xor(s, o, 64);
    const float mean = s * (1.0f / D_);
    float q = 0.f;
    for (int j = lane; j < D_; j += 64) { const float e = bf1(xr[j]) - mean; q += e * e; }
    for (int o = 32; o >= 1; o >>= 1) q += __shfl_xor(q, o, 64);
    const float m = rbf(mean), sd = rbf(sqrtf(rbf(rbf(q * (1.0f / D_)) + 1e-5f)));
    for (int j = lane; j < D_; j += 64) xn[r * D_ + j] = (u16)(pack2(rbf(bf1(xr[j]) - m) / sd, 0.f) & 0xffffu);
  }
  SYNC();
  const int row = tid >> 3, sub = tid & 7;
  float acc[10];
#pragma unroll
  for (int c = 0; c < 10; c++) acc[c] = 0.f;
#pragma unroll 1
  for (int f0 = 0; f0 < F_; f0 += 64) {
    float g[8], u[8];
#pragma unroll
    for (int j = 0; j < 8; j++) { g[j] = 0.f; u[j] = 0.f; }
#pragma unroll 2
    for (int k = 0; k < D_; k++) {
      const float a = bf1(xn[row * D_ + k]);
      const u32x4 wa = *(const u32x4*)(wg + k * F_ + f0 + 8 * sub), wb = *(const u32x4*)(wu + k * F_ + f0 + 8 * sub);
#pragma unroll
      for (int j = 0; j < 4; j++) {
        g[2 * j] = fmaf(a, bflo(wa[j]), g[2 * j]); g[2 * j + 1] = fmaf(a, bfhi(wa[j]), g[2 * j + 1]);
        u[2 * j] = fmaf(a, bflo(wb[j]), u[2 * j]); u[2 * j + 1] = fmaf(a, bfhi(wb[j]), u[2 * j + 1]);
      }
    }
#pragma unroll
    for (int j = 0; j < 4; j++)
      *(unsigned*)(H + row * 64 + 8 * sub + 2 * j) = pack2(rbf(g[2 * j]) * rbf(u[2 * j]), rbf(g[2 * j + 1]) * rbf(u[2 * j + 1]));
    SYNC();
#pragma unroll 2
    for (int f = 0; f < 64; f++) {
      const float a = bf1(H[row * 64 + f]);
      const u16* wr = wd + (f0 + f) * D_ + n0 + 10 * sub;
#pragma unroll
      for (int c = 0; c < 5; c++) {
        const unsigned p = *(const unsigned*)(wr + 2 * c);
        acc[2 * c] = fmaf(a, bflo(p), acc[2 * c]);
        acc[2 * c + 1] = fmaf(a, bfhi(p), acc[2 * c + 1]);
      }
    }
    SYNC();
  }
#pragma unroll 1
  for (int c = 0; c < 80; c++) {
    const int d = n0 + c;
    for (int t = tid; t < m0 + 64; t += 512) {
      float a = 0.f;
#pragma unroll
      for (int j = 0; j < 4; j++) {
        const int ti = t + j - 3;
        a = fmaf(ti >= 0 ? bf1(kin[ti * D_ + d]) : 0.f, bf1(sw[4 * d + 3 - j]), a);
      }
      kvs[t] = rbf(rbf(a) * bf1(vin[t * D_ + d]));
    }
    SYNC();
    const int T = m0 + lane;
    float y = 0.f;
    for (int s = w8; s <= T; s += 8) y = fmaf(bf1(Kf[d * S_ + s]), kvs[T - s], y);
    red[w8 * 64 + lane] = y;
    SYNC();
    if (tid < 64) {
      float ys = 0.f;
#pragma unroll
      for (int j = 0; j < 8; j++) ys += red[j * 64 + lane];
      Z[lane * 80 + c] = (u16)(pack2(rbf(ys) + rbf(kvs[T] * bf1(Dv[d])), 0.f) & 0xffffu);
    }
    SYNC();
  }
#pragma unroll
  for (int c = 0; c < 10; c++) {
    const int col = 10 * sub + c, o = (m0 + row) * D_ + n0 + col;
    const float a = rbf(bf1(xn[row * D_ + n0 + col]) * bf1(Z[row * 80 + col]));
    out[o] = (u16)(pack2(rbf(bf1(x[o]) + a) + rbf(acc[c]), 0.f) & 0xffffu);
  }
}
#define OUTST 0
#define BSC 1
#define TSC 0
#define GT2 gtouch
#define F1E 1
#define F1S 4
#define XMAP 0
#define TLBW 0
#define F3W 1
#define F3S 8
#define F2P 0
#define F3D 2
#define F2D 16
#define FIN8 0
#define SF3 8
#define SF2 8
#define SFS 4
#define RED1 0
#define BL2 0
#define BW 0
#define KVP 3
#define KVJ 1
#define T1N 3
#define KSPLIT 0
#define KVW 2
#define TS 0
#define T2N 3
#define BT2 0
#define ATOUCH 0
#define FPT 4
#define NOPK 2
#define BEARLY 0
#define PRIO_G 2
#define BTOUCH 0
#define PRIO_M 2
static __device__ __attribute__((noinline)) void cw_code_end() { asm volatile("s_nop 0"); }
extern "C" __global__ __launch_bounds__(512) void cw_all(const u16* __restrict__ x, const u16* __restrict__ wg,
                                                         const u16* __restrict__ wu, char* __restrict__ ws,
                                                         const u16* __restrict__ Kf, const u16* __restrict__ wd,
                                                         const u16* __restrict__ kin, const u16* __restrict__ vin,
                                                         const u16* __restrict__ sw, const u16* __restrict__ Dv,
                                                         u16* __restrict__ out) {
  extern __shared__ __attribute__((aligned(16))) char smem[];
  const int tid = threadIdx.x, lane = tid & 63;
  const int w8 = __builtin_amdgcn_readfirstlane(tid >> 6);
  const int bx = blockIdx.x, xcd = bx & 7, idx = bx >> 3;
  const int mp = (xcd & 1) * 8 + (idx & 7), nw = (xcd >> 1) * 4 + (idx >> 3);
  const int m0 = mp * 128, n0 = (108 * nw) & ~7;
  const int odd = nw & 1;
  constexpr int NKT = D_ / 64;
  const unsigned ep0 = (unsigned)cw_dispatch_id() * 2654435761u + 0x6a09e667u;
  unsigned salt;
  const int kt0 = 64 * ((bx & 7) * 4 + ((bx >> 3) & 3)), kd0 = 80 * (bx >> 5);
  const int ce = bx & 1, cd2 = 5 * (bx >> 1) + 2, cd0 = cd2 - 2 + 3 * ce;
  const int mh = (xcd >> 1) & 1, nt = (xcd >> 2) * 4 + (idx >> 3), ts8 = idx & 7;
  const int om0 = m0 + 64 * mh, on0 = nt * 80;
  const unsigned sm0 = lds_off(smem);
  const int g = lane >> 4, r16 = lane & 15;
  STAMP_DECL
  STAMP(0); STAMPR(30);
  u32x4 sq[3];
  bool bad = false;
  if (w8 >= 4) {
    const int w = w8 - 4, pt = tid - 256;
    const bool tw = (w == 1) || (w == 2);
    const u16* tb = tw ? (w == 1 ? wg : wu) : x;
    auto tvo = [&](bool isw, int first, int n) {
      int sl = isw ? lane / 7 : lane >> 3;
      sl = sl < n - 1 ? sl : n - 1;
      const int ln = 7 * bx + lane % 7;
      const unsigned b0 = isw ? (ln < 1727 ? ln : 1727) * 128 : (8 * bx + (lane & 7)) * (D_ * 2);
      return b0 + (first + sl) * (isw ? 64 * F_ * 2 : 128);
    };
    gtouch(tvo(tw, 1, T1N), tb, sm0 + L_DUMP);
    const unsigned tv2 = tvo(tw, T1N + 1, TS == 1 ? T2N : 9 - T1N);
    unsigned asrc[4];
    const u16* bsrc[7];
#pragma unroll
    for (int i = 0; i < 4; i++) {
      int slot = (w * 4 + i) * 64 + lane, row = slot >> 3, chunk = (slot & 7) ^ ((row >> 1) & 7);
      asrc[i] = ((m0 + row) * D_ + chunk * 8) * 2;
    }
    {
      const u16* wb = ((lane & 1) ? wu : wg) + n0;
      const int q28 = (lane >= 28) + (lane >= 56);
      int kk = 16 * w + q28, p = lane - 28 * q28;
#pragma unroll
      for (int i = 0; i < 7; i++) {
        const int tau = (p >> 1) ^ ((kk >> 3) & 1);
        bsrc[i] = wb + (unsigned)(kk * F_ + 8 * tau);
        p += 8; kk += 2;
        if (p >= 28) { p -= 28; kk++; }
      }
    }
    auto issue = [&](int kt, int dq) {
      const unsigned base = sm0 + (kt % 3) * G1_STAGE;
#pragma unroll
      for (int i = 0; i < 4; i++) glds16(asrc[i], x + kt * 64 - dq * 32 * D_, base + ((w - dq) * 4 + i) * 1024);
#pragma unroll
      for (int i = 0; i < 7; i++) glds16v(bsrc[i] + (kt * 64 - dq * 16) * F_, base + G1_A + ((w - dq) * 7 + i) * 1024);
    };
    unsigned dw1, dw2;
    asm volatile("s_load_dword %0, %1, 0x0" : "=s"(salt) : "s"(ws + WS_G) : "memory");
    asm volatile("s_load_dword %0, %1, 0x0" : "=s"(dw1) : "s"(Dv + ((cd0 + (w >> 1)) & ~1)) : "memory");
    asm volatile("s_load_dword %0, %1, 0x0" : "=s"(dw2) : "s"(Dv + (cd2 & ~1)) : "memory");
    issue(0, 0);
    issue(1, 0);
    asm volatile("s_waitcnt vmcnt(11) lgkmcnt(0)" : "+s"(salt), "+s"(dw1), "+s"(dw2)::"memory");
    const unsigned ep = ep0 + salt * 0x9e3779b5u;
    STAMP(1);
    BAR();
    issue(2, 0);
    if (TS != 2) gtouch(tv2, tb, sm0 + L_DUMP);
    constexpr int T2 = TS != 2;
    unsigned fpa = 0;
    if (w >= 2) {
      const bool pkw = bx < 224;
      const int pnt = bx / 28, pks = 2 * (bx - 28 * pnt) + (w & 1);
      unsigned psrc[5];
#pragma unroll
      for (int i = 0; i < 5; i++) {
        const int c = i * 64 + lane, kq = 32 * pks + c / 10;
        const int tn = kq / 112;
        psrc[i] = ((((108 * tn) & ~7) + kq - 112 * tn) * D_ + 80 * pnt + 8 * (c % 10)) * 2;
        asm volatile("" : "+v"(psrc[i]));
      }
      waitv<11 + T2>();
      BAR();
      STAMP(2);
      issue(3, 0); issue(3, 2);
      waitv<22 + T2>();
      BAR();
      STAMP(3);
#pragma unroll 1
      for (int kt = 4; kt < 9; kt++) {
        issue(kt, 0); issue(kt, 2);
        waitv<22>();
        BAR();
      }
      STAMP(8);
      issue(9, 0); issue(9, 2);
      CvK Kc;
      cv_ldk(Kc, cd2, pt - 128, Kf);
      if (pkw) {
#pragma unroll
        for (int i = 0; i < 5; i++) glds16n(psrc[i], wd, sm0 + L_SP + (w & 1) * 5120 + i * 1024);
        waitv<30>();
      } else {
        waitv<25>();
      }
      BAR();
      STAMP(9);
      if (pkw) waitv<8>();
      else waitv<3>();
      ct_add(smem, C_S9, lane);
      asm volatile("s_waitcnt vmcnt(0)" : "+v"(Kc.lo), "+v"(Kc.h0), "+v"(Kc.h1)::"memory");
      if (pkw) {
        const char* pp = smem + L_SP + (w & 1) * 5120 + (8 * g + (r16 >> 2)) * 160 + (lane & 3) * 8;
        const int k0 = 32 * pks + 8 * g, tn = k0 / 112, j0 = k0 - 112 * tn;
        const bool zl = (tn & 1) && j0 == 0, zh = !(tn & 1) && j0 == 104;
#pragma unroll
        for (int tt = 0; tt < 5; tt++) {
          const u32x2 lo = __builtin_bit_cast(u32x2, trread(pp + tt * 32)), hi = __builtin_bit_cast(u32x2, trread(pp + tt * 32 + 640));
          gst16c(WS_WDP + (pnt * 56 + pks) * 5120 + lane * 16, ws + tt * 1024,
                 (u32x4){zl ? 0u : lo[0], zl ? 0u : lo[1], zh ? 0u : hi[0], zh ? 0u : hi[1]});
        }
      }
      WAIT_L0();
      ct_add(smem, C_RD, lane);
      STAMP(5);
      ct_wait(smem, C_RD, 2);
      cv_build(Kc, smem + L_SP, pt - 128);
      WAIT_L0();
      ct_add(smem, C_B2, lane);
      fpa = pkw ? WS_FP + FSP * (pnt * 56 + pks) : 0u;
      STAMP(6);
    } else {
      unsigned ksrc[11];
      {
        int row = lane / 10, pc = lane - 10 * row;
        row += kt0 - (w ? 0 : 3);
#pragma unroll
        for (int i = 0; i < 11; i++) {
          int rr = row > 0 ? row : 0;
          rr = rr < kt0 + 63 ? rr : kt0 + 63;
          ksrc[i] = (rr * D_ + kd0 + 8 * pc) * 2;
          asm volatile("" : "+v"(ksrc[i]));
          row += 6; pc += 4;
          if (pc >= 10) { pc -= 10; row++; }
        }
      }
      waitv<11 + T2>();
      BAR();
      STAMP(2);
      const u16* kb = w ? vin : kin;
      CvK Ka, Kb;
#pragma unroll
      for (int i = 0; i < 10; i++) glds16n(ksrc[i], kb, sm0 + L_SP + w * KVS_V + i * 1024);
      if (w) glds16((4 * kd0 + 8 * (lane < 40 ? lane : 0)) * 2, sw, sm0 + L_SP + KVS_W);
      else glds16n(ksrc[10], kb, sm0 + L_SP + 10240);
      waitv<11 + T2>();
      BAR();
      STAMP(3);
      cv_ldk(Ka, cd0, pt, Kf);
      cv_ldk(Kb, cd0 + 1, pt, Kf);
      waitv<6>();
      STAMP(4);
      if (TS == 1) {
        gtouch(tvo(tw, T1N + T2N + 1, 9 - T1N - T2N), tb, sm0 + L_DUMP);
        if (w == 0) gtouch(tvo(true, T1N + T2N + 1, 9 - T1N - T2N), wu, sm0 + L_DUMP);
      }
      BAR();
      const char* tl = smem + L_SP + (3 + 8 * g + (r16 >> 2) + 16 * (lane & 2)) * 160 + (lane & 1) * 8 + w * 80;
      const unsigned kvo = WS_KV + ((kd0 + 40 * w + (r16 & 7)) * S_ + 2040 - kt0 - 4 * (r16 & 8) - 8 * g) * 2;
      auto kvjob = [&](int u) {
        const char* p = tl + u * 16;
        u32x2 kp = __builtin_bit_cast(u32x2, trread(p - 640));
        const u32x2 kl = __builtin_bit_cast(u32x2, trread(p)), kh = __builtin_bit_cast(u32x2, trread(p + 640));
        const u32x2 vl = __builtin_bit_cast(u32x2, trread(p + KVS_V - 480)), vh = __builtin_bit_cast(u32x2, trread(p + KVS_V + 160));
        const u32x2 wv = *(const u32x2*)(smem + L_SP + KVS_W + 8 * (40 * w + 8 * u + (r16 & 7)));
        if (kt0 + (r16 & 8) + g == 0) kp = (u32x2){0u, 0u};
        gst16c(kvo, ws + u * (8 * S_ * 2), kv8(kp, kl, kh, vl, vh, wv));
      };
      if (KVP) __builtin_amdgcn_s_setprio(KVP);
      kvjob(0);
      kvjob(1);
      kvjob(2);
      BAR();
      kvjob(3);
      kvjob(4);
      BAR();
      if (KVP) __builtin_amdgcn_s_setprio(0);
      asm volatile("s_waitcnt vmcnt(5) lgkmcnt(0)" : "+v"(Ka.lo), "+v"(Ka.h0), "+v"(Ka.h1), "+v"(Kb.lo), "+v"(Kb.h0), "+v"(Kb.h1)::"memory");
      STAMP(5);
      bool posted = false;
#pragma unroll 1
      for (int b = KVW + 3 + (KVJ == 3 ? 2 : KVJ == 4); b < 9 - F1E; b++) {
        if (!posted) {
          bool go = b == 8 - F1E;
#pragma unroll 1
          for (int t = 0; t < 4 && !go; t++) {
            go = (__builtin_amdgcn_s_getreg((31 << 11) | 7) & 0xc0000f) == 0;
            if (!go) __builtin_amdgcn_s_sleep(4);
          }
          if (go) {
            waitv<0>();
            posted = true;
            if (ct_add(smem, C_F1, lane) == 1 && lane == 0) gst4c(WS_F1 + FS1 * bx, ws, ep);
            STAMP(7);
          }
        }
        if (b < 8 - F1E) BAR();
      }
      const unsigned f1o = WS_F1 + FS1 * ((bx >> 5) * 32 + (lane & 31));
      unsigned fl;
      __builtin_amdgcn_s_sleep(F1S);
      asm volatile("global_load_dword %0, %1, %2 sc1" : "=v"(fl) : "v"(f1o), "s"(ws) : "memory");
      BAR();
      STAMP(9);
      asm volatile("s_waitcnt vmcnt(0)" : "+v"(fl)::"memory");
      STAMP(6);
      bool ok1 = __builtin_amdgcn_ballot_w64(fl != ep) == 0;
      if (!ok1) ok1 = poll_flags<1>(ws, f1o, ep);
      STAMP(10);
      if (ok1) {
        const unsigned vo = WS_KV + w * 2048 + lane * 16;
#pragma unroll
        for (int i = 0; i < 2; i++) {
          glds16c(vo, ws + cd0 * (S_ * 2) + i * 1024, sm0 + CV_R0 + CV_UO + w * 2048 + i * 1024);
          glds16c(vo, ws + (cd0 + 1) * (S_ * 2) + i * 1024, sm0 + CV_R0 + CV_R + CV_UO + w * 2048 + i * 1024);
          glds16c(vo, ws + cd2 * (S_ * 2) + i * 1024, sm0 + L_SP + CV_UO + w * 2048 + i * 1024);
        }
      } else {
        ct_add(smem, C_BAD1, lane);
        ct_add(smem, C_BAD, lane);
      }
      if (F1E) cv_build(Ka, smem + CV_R0, pt);
      cv_build(Kb, smem + CV_R0 + CV_R, pt);
      asm volatile("s_waitcnt vmcnt(0) lgkmcnt(0)" ::: "memory");
      ct_add(smem, C_BUILD, lane);
    }
    ct_wait(smem, C_BUILD, 2);
    STAMP(11);
    const bool bad1 = ct_get(smem, C_BAD1) != 0;
    if (!bad1) {
      const int ch = w >> 1, hh = w & 1;
      const char* reg = smem + CV_R0 + ch * CV_R;
      f32x4 acc[8], kp[4];
      cv_half(reg, lane, 4 * hh, acc, fpa, ws, ep);
      fpa = 0;
      STAMP(12);
      ct_wait(smem, C_LOOP, 4);
      char* pw = smem + CV_P + w * 4096 + lane * 16;
      if (hh) {
#pragma unroll
        for (int n = 0; n < 4; n++) { *(f32x4*)(pw + n * 1024) = acc[n]; kp[n] = acc[4 + n]; }
      } else {
#pragma unroll
        for (int n = 0; n < 4; n++) { *(f32x4*)(pw + n * 1024) = acc[4 + n]; kp[n] = acc[n]; }
      }
      ct_wait(smem, C_B2, 2);
      {
        f32x4 a2[4];
        if (ce) cv_part<1>(smem + L_SP, lane, 2 * w, a2);
        else cv_part<0>(smem + L_SP, lane, 2 * w, a2);
        char* p2 = smem + CV_P + 16384 + w * 1024 + lane * 16;
#pragma unroll
        for (int b = 0; b < 4; b++) *(f32x4*)(p2 + b * 4096) = a2[b];
      }
      WAIT_L0();
      ct_add(smem, C_PA, lane);
      STAMP(13);
      const float dd = ((cd0 + ch) & 1) ? bfhi(dw1) : bflo(dw1), d2 = (cd2 & 1) ? bfhi(dw2) : bflo(dw2);
      ct_wait(smem, C_PA, 4);
      const char* pr = smem + CV_P + (w ^ 1) * 4096 + lane * 16;
      const int t0 = 1024 * hh + 16 * r16 + 4 * g;
      u32x2 z[4];
#pragma unroll
      for (int n = 0; n < 4; n++) z[n] = cv_z(kp[n] + *(const f32x4*)(pr + n * 1024), reg + CV_UO + 2 * (2044 - t0 - 256 * n), dd);
      const unsigned zo = WS_Z + ((cd0 + ch) * S_ + 1024 * hh + 256 * (g & 1) + 16 * r16 + 8 * (g >> 1)) * 2;
#pragma unroll
      for (int n = 0; n < 4; n += 2) {
        const u32x2 a = __builtin_amdgcn_permlane16_swap(z[n][0], z[n + 1][0], false, false);
        const u32x2 b = __builtin_amdgcn_permlane16_swap(z[n][1], z[n + 1][1], false, false);
        gst16t<NTST & 8>(zo + 512 * n, ws, (u32x4){a[0], b[0], a[1], b[1]});
      }
      const char* pp = smem + CV_P + 16384 + w * 4096 + lane * 16;
      const f32x4 s = (*(const f32x4*)pp + *(const f32x4*)(pp + 1024)) + (*(const f32x4*)(pp + 2048) + *(const f32x4*)(pp + 3072));
      const int t2 = 256 * (ce ? 5 - w : (w < 2 ? 7 - w : 3 - w)) + 16 * r16 + 4 * g;
      gst8c(WS_Z + (cd2 * S_ + t2) * 2, ws, cv_z(s, smem + L_SP + CV_UO + 2 * (2044 - t2), d2));
      STAMP(14);
    }
    if (fpa) {
      waitv<0>();
      if (lane == 0) gst4c(fpa, ws, ep);
    }
    WAIT_L0();
    ct_add(smem, C_CV, lane);
    u32x4 xv[3], xn[3];
#pragma unroll
    for (int r = 0; r < 3; r++) xv[r] = gload16(x + (unsigned)((om0 + g2_row(r, w, lane)) * D_ + on0 + 8 * g2_pc(r, w, lane)));
    asm volatile("s_waitcnt vmcnt(0)" : "+v"(xv[0]), "+v"(xv[1]), "+v"(xv[2])::"memory");
    STAMP(15);
    if (!bad1 && ct_add(smem, C_Z, lane) == 3 && lane == 0) gst4c(WS_F3 + FS3 * bx, ws, ep);
    ct_wait(smem, C_H, 4);
#pragma unroll
    for (int r = 0; r < 3; r++) {
      const f32x2 ms = *(const f32x2*)(smem + L_MS + 8 * (64 * mh + g2_row(r, w, lane)));
      xn[r] = ln8(xv[r], ms[0], ms[1]);
    }
    asm volatile("" : "+v"(xn[0]), "+v"(xn[1]), "+v"(xn[2]));
    STAMP(16);
    if (bx == 0 && w == 1) {
      bool al = true;
#pragma unroll 1
      for (int i = 0; i < 4 && al; i++) al = poll_flags<1, 1>(ws, WS_F1 + FS1 * (64 * i + lane), ep);
      if (al && lane == 0) gst4c(WS_G, ws, salt + 1u);
    }
    if (w == 0) {
#pragma unroll 1
      for (int i = 0; i < F3D; i++) __builtin_amdgcn_s_sleep(16);
      bool ok3 = false;
#pragma unroll 1
      for (int i = 0; i < SF3 && !ok3 && !bad1; i++) {
        ok3 = sflags32(ws + WS_F3 + 128 * 32 * nt, ep);
        if (!ok3) __builtin_amdgcn_s_sleep(SFS);
      }
      STAMP(29);
      if (bad1 || !(ok3 || poll_flags<1>(ws, WS_F3 + FS3 * (nt * 32 + (lane & 31)), ep))) ct_add(smem, C_BAD, lane);
      ct_add(smem, C_F3K, lane);
    }
    ct_wait(smem, C_F3K, 1);
    {
      const unsigned zsr = WS_Z + ((on0 + (lane >> 3)) * S_ + om0) * 2 + (lane & 7) * 16;
#pragma unroll
      for (int i = 0; i < 3; i++) {
        const int q = (w + 4 * i) < 10 ? w + 4 * i : w;
        glds16c(zsr, ws + q * (8 * S_ * 2), sm0 + G2_ZS + q * 1024);
      }
    }
    STAMP(17);
    asm volatile("s_waitcnt vmcnt(0) lgkmcnt(0)" ::: "memory");
    ct_add(smem, C_ZL, lane);
    ct_wait(smem, C_ZL, 4);
    STAMP(19);
#pragma unroll
    for (int r = 0; r < 3; r++) {
      const char* p = smem + G2_ZS + (8 * g2_pc(r, w, lane) + (r16 >> 2)) * 128 + 2 * (g2_row(r, w, lane) - r16) + 8 * (r16 & 3);
      const u32x2 zl = __builtin_bit_cast(u32x2, trread(p)), zh = __builtin_bit_cast(u32x2, trread(p + 512));
      sq[r] = (u32x4){sum2(xv[r][0], xn[r][0], zl[0]), sum2(xv[r][1], xn[r][1], zl[1]), sum2(xv[r][2], xn[r][2], zh[0]),
                      sum2(xv[r][3], xn[r][3], zh[1])};
      if (FIN8) *(u32x4*)(smem + L_SP + (r * 4 + w) * 1024 + lane * 16) = sq[r];
    }
  } else {
    const int w = w8;
    __builtin_amdgcn_s_setprio(PRIO_M);
    asm volatile("s_load_dword %0, %1, 0x0" : "=s"(salt) : "s"(ws + WS_G) : "memory");
    if (w == 0) {
      *(unsigned*)(smem + L_CT + 4 * lane) = 0u;
      unsigned long long pc;
      int dk;
      asm volatile("s_getpc_b64 %0\n.Lcw_here:\n\ts_mov_b32 %1, .Lcw_here-cw_all" : "=s"(pc), "=s"(dk));
      const unsigned long long c0 = pc - (long long)dk, c1 = (unsigned long long)(uintptr_t)&cw_code_end;
      const long long len = (long long)(c1 - c0);
      const unsigned off = (unsigned)(idx * 12 + lane) * 128;
      if (lane < 12 && (long long)off < len) gtouch(off, (const void*)c0, sm0 + L_DUMP);
    }
    unsigned fpv;
    const int wr = w >> 1, wc = w & 1;
    const int q = r16 >> 2, p = lane & 3;
    {
    int aptr[2];
#pragma unroll
    for (int ks = 0; ks < 2; ks++) {
      int row = wr * 64 + r16, chunk = ks * 4 + g;
      aptr[ks] = (row * 8 + (chunk ^ ((row >> 1) & 7))) * 16;
    }
    int taus[7];
#pragma unroll
    for (int tt = 0; tt < 7; tt++) {
      int j = (tt == 0) ? 0 : 1 + ((tt - 1 + 3 * wr) % 6);
      taus[tt] = wc ? 13 - j : j;
    }
    int bptr[7];
#pragma unroll
    for (int tt = 0; tt < 7; tt++)
      bptr[tt] = G1_A + (28 * (8 * g + q) + 2 * (taus[tt] ^ (g & 1)) + (p >> 1)) * 16 + 8 * (p & 1);
    const bool patch = (wc != odd) && (r16 == (odd ? 0 : 7));
    const int arot = wc * 4096;

    f32x4 acc[4][7], accg[2], accc[4];
#pragma unroll
    for (int i = 0; i < 4; i++)
#pragma unroll
      for (int j = 0; j < 7; j++) acc[i][j] = (f32x4){0.f, 0.f, 0.f, 0.f};
#pragma unroll
    for (int i = 0; i < 2; i++) accg[i] = (f32x4){0.f, 0.f, 0.f, 0.f};
#pragma unroll
    for (int i = 0; i < 4; i++) accc[i] = (f32x4){0.f, 0.f, 0.f, 0.f};
    const bf16x8 ones = {0x3F80, 0x3F80, 0x3F80, 0x3F80, 0x3F80, 0x3F80, 0x3F80, 0x3F80};
    int ap0[2] = {aptr[0] + arot, aptr[1] + arot};
    int ap1[2] = {aptr[0] + (arot ^ 4096), aptr[1] + (arot ^ 4096)};

    bf16x8 af0[4], bf0[7], af1[4], bf1_[7];
    auto rd = [&](int ks, bf16x8* af, bf16x8* bfr) {
#pragma unroll
      for (int tt = 0; tt < 7; tt++) {
        s16x4 lo = trread(smem + bptr[tt] + ks * 32 * 448);
        s16x4 hi = trread(smem + bptr[tt] + ks * 32 * 448 + 4 * 448);
        bfr[tt] = __builtin_shufflevector(lo, hi, 0, 1, 2, 3, 4, 5, 6, 7);
      }
      af[0] = *(const bf16x8*)(smem + ap0[ks]);
      af[1] = *(const bf16x8*)(smem + ap0[ks] + 2048);
      af[2] = *(const bf16x8*)(smem + ap1[ks]);
      af[3] = *(const bf16x8*)(smem + ap1[ks] + 2048);
    };
    auto mm = [&](bf16x8* af, bf16x8* bfr) {
      bf16x8 b0 = patch ? ones : bfr[0];
#pragma unroll
      for (int rt = 0; rt < 4; rt++)
#pragma unroll
        for (int tt = 1; tt < 7; tt++) acc[rt][tt] = MFMA16(bfr[tt], af[rt], acc[rt][tt]);
#pragma unroll
      for (int rt = 0; rt < 4; rt++) acc[rt][0] = MFMA16(b0, af[rt], acc[rt][0]);
#pragma unroll
      for (int j = 0; j < 2; j++) accg[j] = MFMA16(af[j], af[j], accg[j]);
      accc[0] = MFMA16(b0, ones, accc[0]);
#pragma unroll
      for (int j = 1; j < 4; j++) accc[j] = MFMA16(bfr[j], ones, accc[j]);
    };
    auto sched = [&]() {
#pragma unroll
      for (int i = 0; i < 9; i++) { SGB(0x8, 1); SGB(0x100, 1); SGB(0x8, 1); SGB(0x100, 1); SGB(0x8, 1); SGB(0x2, 1); }
      SGB(0x8, 7);
    };
    asm volatile("s_waitcnt lgkmcnt(0)" : "+s"(salt)::"memory");
    STAMP(1);
    BAR();
    STAMP(2);
    rd(0, af0, bf0);
    int buf = 0;
#pragma unroll 1
    for (int kt = 0; kt < NKT; kt++) {
      const int delta = (buf == 2) ? -2 * G1_STAGE : G1_STAGE;
      buf = (buf == 2) ? 0 : buf + 1;
      SB0();
      rd(1, af1, bf1_);
      mm(af0, bf0);
      sched();
      SB0();
      WAIT_L0();
      if (kt < 8) BAR();
      else if (kt == 8) ct_wait(smem, C_S9, 2);
      SB0();
#pragma unroll
      for (int tt = 0; tt < 7; tt++) bptr[tt] += delta;
      ap0[0] += delta; ap0[1] += delta; ap1[0] += delta; ap1[1] += delta;
      rd(0, af0, bf0);
      mm(af1, bf1_);
      sched();
      SB0();
    }
    WAIT_L0();
    STAMP(9);
    ct_add(smem, C_LOOP, lane);
    {
      const int e = r16 & 3;
#pragma unroll
      for (int j = 0; j < 2; j++) {
        const float lo = (e & 1) ? accg[j][1] : accg[j][0], hi = (e & 1) ? accg[j][3] : accg[j][2];
        if (q == g) *(float*)(smem + L_RQ + 4 * (wr * 64 + 16 * ((j + 2 * wc) & 3) + r16)) = (e & 2) ? hi : lo;
      }
      if (wc != odd && g == (odd ? 0 : 1)) {
#pragma unroll
        for (int rt = 0; rt < 4; rt++)
          *(float*)(smem + L_RS + 4 * (wr * 64 + 16 * ((rt + 2 * wc) & 3) + r16)) = odd ? acc[rt][0][0] : acc[rt][0][3];
      }
      if (r16 == 0) {
#pragma unroll
        for (int j = 0; j < 4; j++) *(f32x4*)(smem + L_CS + taus[j] * 64 + 16 * g) = accc[j];
      }
    }
    WAIT_L0();
    ct_add(smem, C_STAT, lane);
    ct_wait(smem, C_STAT, 4);
    STAMP(10);
    float mi[4], ir[4];
    f32x4 cs[7];
#pragma unroll
    for (int tt = 0; tt < 7; tt++) cs[tt] = *(const f32x4*)(smem + L_CS + taus[tt] * 64 + 16 * g);
#pragma unroll
    for (int rt = 0; rt < 4; rt++) {
      const int row = wr * 64 + 16 * ((rt + 2 * wc) & 3) + r16;
      const float rq = *(const float*)(smem + L_RQ + 4 * row), rs = *(const float*)(smem + L_RS + 4 * row);
      const float mean = rs * (1.0f / D_);
      const float var = fmaxf(rq * (1.0f / D_) - mean * mean, 0.f);
      const float mm_ = rbf(mean);
      const float sd_ = rbf(__builtin_amdgcn_sqrtf(rbf(rbf(var) + 1e-5f)));
      ir[rt] = __builtin_amdgcn_rcpf(sd_);
      mi[rt] = -mm_ * ir[rt];
      if (wc == 0 && g == 0) *(f32x2*)(smem + L_MS + 8 * row) = (f32x2){mm_, sd_};
    }
    f32x2 mi2[4], ir2[4];
#pragma unroll
    for (int rt = 0; rt < 4; rt++) {
      mi2[rt] = (f32x2){mi[rt], mi[rt]};
      ir2[rt] = (f32x2){ir[rt], ir[rt]};
    }
    const unsigned hb = WS_H + ((mp * 8 + wr * 4 + ((((g & 1) << 1) + (g >> 1) + 2 * wc) & 3)) * 224 + nw * 14) * 256 + r16 * 16;
    const bool zpad = (wc != odd) && ((g & 1) != odd);
    const unsigned fpo = WS_FP + FSP * (nt * 56 + 14 * w + (r16 < 14 ? r16 : 0));
#pragma unroll
    for (int tt = 0; tt < 7; tt++) {
      u32x2 o[4];
#pragma unroll
      for (int rt = 0; rt < 4; rt++) {
        f32x4 v;
#pragma unroll
        for (int e = 0; e < 4; e += 2) {
          f32x2 t, r;
          asm("v_pk_mul_f32 %0, %1, %2" : "=v"(t) : "v"((f32x2){cs[tt][e], cs[tt][e + 1]}), "v"(mi2[rt]));
          asm("v_pk_fma_f32 %0, %1, %2, %3" : "=v"(r) : "v"((f32x2){acc[rt][tt][e], acc[rt][tt][e + 1]}), "v"(ir2[rt]), "v"(t));
          v[e] = r[0]; v[e + 1] = r[1];
        }
        o[rt] = (u32x2){pack2(v[0], v[1]), pack2(v[2], v[3])};
      }
      unsigned hh[2][2];
#pragma unroll
      for (int pr = 0; pr < 2; pr++)
#pragma unroll
        for (int i = 0; i < 2; i++) {
          const u32x2 s = __builtin_amdgcn_permlane32_swap(o[2 * pr][i], o[2 * pr + 1][i], false, false);
          f32x2 pp;
          asm("v_pk_mul_f32 %0, %1, %2" : "=v"(pp) : "v"((f32x2){bflo(s[0]), bfhi(s[0])}), "v"((f32x2){bflo(s[1]), bfhi(s[1])}));
          hh[pr][i] = pack2(pp[0], pp[1]);
          if (tt == 0 && zpad) hh[pr][i] = 0u;
        }
      const u32x2 a = __builtin_amdgcn_permlane16_swap(hh[0][0], hh[1][0], false, false);
      const u32x2 b = __builtin_amdgcn_permlane16_swap(hh[0][1], hh[1][1], false, false);
      gst16t<NTST & 4>(hb, ws + taus[tt] * 256, (u32x4){a[0], b[0], a[1], b[1]});
      if (tt == FPT) asm volatile("global_load_dword %0, %1, %2 sc1" : "=v"(fpv) : "v"(fpo), "s"(ws) : "memory");
    }
    asm volatile("s_waitcnt vmcnt(%1)" : "+v"(fpv) : "n"(6 - FPT) : "memory");
    }
    STAMP(11);
    const unsigned ep = ep0 + salt * 0x9e3779b5u;
    bool okp = __builtin_amdgcn_ballot_w64(fpv != ep) == 0;
    const bool early = BEARLY && okp;
    if (!early) {
      waitv<0>();
      STAMP(12);
      if (ct_add(smem, C_H, lane) == 3 && lane == 0) gst4c(WS_F2 + F2M * mp + FS2 * nw, ws, ep);
      if (!okp) okp = poll_flags<1>(ws, WS_FP + FSP * (nt * 56 + 14 * w + (r16 < 14 ? r16 : 0)), ep);
    }
    if (BW) ct_wait(smem, C_H, 4);
    STAMP(13);
    if (!okp) {
      ct_add(smem, C_BAD, lane);
      ct_add(smem, C_BR, lane);
    } else {
      f32x4 acc[4][5];
#pragma unroll
      for (int i = 0; i < 4; i++)
#pragma unroll
        for (int j = 0; j < 5; j++) acc[i][j] = (f32x4){0.f, 0.f, 0.f, 0.f};
      unsigned ao[4];
#pragma unroll
      for (int rt = 0; rt < 4; rt++) ao[rt] = WS_H + ((mp * 8 + mh * 4 + rt) * 224 + 56 * w) * 256 + lane * 16;
      const unsigned bo = WS_WDP + (nt * 56 + 14 * w) * 5120 + 2048 + lane * 16;
      bf16x8 A[4][4], B[4][5];
#define G2_LA(ks) do { _Pragma("unroll") for (int rt = 0; rt < 4; rt++) A[(ks) & 3][rt] = gl16c<((ks) & 3) * 1024>(ao[rt], ws + ((ks) >> 2) * 4096); } while (0)
#define GLB gl16c
#define G2_LB(ks) do { B[(ks) & 3][0] = GLB<-2048>(bo, ws + (ks) * 5120); B[(ks) & 3][1] = GLB<-1024>(bo, ws + (ks) * 5120); \
                       B[(ks) & 3][2] = GLB<0>(bo, ws + (ks) * 5120); B[(ks) & 3][3] = GLB<1024>(bo, ws + (ks) * 5120); \
                       B[(ks) & 3][4] = GLB<2048>(bo, ws + (ks) * 5120); } while (0)
#define G2_TIE(s) "+v"(A[s][0]), "+v"(A[s][1]), "+v"(A[s][2]), "+v"(A[s][3]), "+v"(B[s][0]), "+v"(B[s][1]), "+v"(B[s][2]), "+v"(B[s][3]), "+v"(B[s][4])
#define G2_TB(s) "+v"(B[s][0]), "+v"(B[s][1]), "+v"(B[s][2]), "+v"(B[s][3]), "+v"(B[s][4])
#define G2_STEP(ks, nw_) do { asm volatile("s_waitcnt vmcnt(%9)" : G2_TIE((ks) & 3) : "n"(nw_) : "memory"); SB0(); \
        _Pragma("unroll") for (int tt = 0; tt < 5; tt++) _Pragma("unroll") for (int rt = 0; rt < 4; rt++) \
          acc[rt][tt] = MFMA16(B[(ks) & 3][tt], A[(ks) & 3][rt], acc[rt][tt]); \
        SB0(); if ((ks) + 4 < 14) { G2_LA((ks) + 4); G2_LB((ks) + 4); } } while (0)
      const unsigned fo = WS_F2 + F2M * mp + FS2 * (4 * w + (lane & 3));
      G2_LB(0); G2_LB(1); G2_LB(2); G2_LB(3);
      __builtin_amdgcn_s_setprio(PRIO_G);
      if (early) {
        waitv<20>();
        STAMP(12);
        if (ct_add(smem, C_H, lane) == 3 && lane == 0) gst4c(WS_F2 + F2M * mp + FS2 * nw, ws, ep);
      }
      bool ok2 = false;
      if (F2D) __builtin_amdgcn_s_sleep(F2D);
#pragma unroll 1
      for (int i = 0; i < SF2 && !ok2; i++) {
        ok2 = sflags4(ws + WS_F2 + F2M * mp + 512 * w, ep);
        if (!ok2) __builtin_amdgcn_s_sleep(SFS);
      }
      STAMP(14);
      if (!ok2) ok2 = poll_flags<1>(ws, fo, ep);
      STAMP(16);
      if (ok2) {
        G2_LA(0); G2_LA(1); G2_LA(2); G2_LA(3);
#define AT_ 0
        G2_STEP(0, 12 + AT_);
        STAMP(17);
        G2_STEP(1, 17 + AT_); G2_STEP(2, 22 + AT_); G2_STEP(3, 27 + AT_); G2_STEP(4, 27); G2_STEP(5, 27); G2_STEP(6, 27);
        G2_STEP(7, 27); G2_STEP(8, 27); G2_STEP(9, 27); G2_STEP(10, 27); G2_STEP(11, 18); G2_STEP(12, 9); G2_STEP(13, 0);
        STAMP(18);
        ct_wait(smem, C_CV, 4);
        if (BL2) ct_wait(smem, C_BR, 4);
        float* red = (float*)smem;
#pragma unroll
        for (int rt = 0; rt < 4; rt++)
#pragma unroll
          for (int tt = 0; tt < 5; tt++) *(f32x4*)(red + (w * 64 + rt * 16 + r16) * 84 + tt * 16 + 4 * g) = acc[rt][tt];
      } else {
        waitv<0>();
        ct_add(smem, C_BAD, lane);
        ct_add(smem, C_BR, lane);
      }
    }
  }
  WAIT_L0();
  STAMP(20);
  BAR();
  STAMP(21);
  if (__builtin_expect(ct_get(smem, C_BAD) != 0, 0)) {
    slow_tile(smem, x, kin, vin, sw, Kf, Dv, wg, wu, wd, out, om0, on0);
    STAMP(25);
  } else if (w8 >= 4) {
    const int w = w8 - 4;
#pragma unroll
    for (int r = 0; r < 3; r++) {
      const float* rp = (const float*)smem + g2_row(r, w, lane) * 84 + 8 * g2_pc(r, w, lane);
      const f32x4 b0 = (*(const f32x4*)rp + *(const f32x4*)(rp + 64 * 84)) + (*(const f32x4*)(rp + 128 * 84) + *(const f32x4*)(rp + 192 * 84));
      const f32x4 b1 = (*(const f32x4*)(rp + 4) + *(const f32x4*)(rp + 64 * 84 + 4)) +
                       (*(const f32x4*)(rp + 128 * 84 + 4) + *(const f32x4*)(rp + 192 * 84 + 4));
      const u32x4 bq = {pack2(b0[0], b0[1]), pack2(b0[2], b0[3]), pack2(b1[0], b1[1]), pack2(b1[2], b1[3])};
      u32x4 o;
#pragma unroll
      for (int i = 0; i < 4; i++) o[i] = pack2(bflo(sq[r][i]) + bflo(bq[i]), bfhi(sq[r][i]) + bfhi(bq[i]));
      const unsigned oo = ((om0 + g2_row(r, w, lane)) * D_ + on0 + 8 * g2_pc(r, w, lane)) * 2;
      if (r < 2 || lane < 32) {
        *(u32x4*)((char*)out + oo) = o;
      }
    }
  }
  STAMP(22);
  STAMP(23); STAMPR(31);
  STAMP_OUT;
}

extern "C" void kernel_launch(void* const* d_in, const int* in_sizes, int n_in, void* d_out, int out_size,
                              void* d_ws, size_t ws_size, hipStream_t stream) {
  const u16* x = (const u16*)d_in[0];
  const u16* k = (const u16*)d_in[1];
  const u16* v = (const u16*)d_in[2];
  const u16* sw = (const u16*)d_in[3];
  const u16* Kf = (const u16*)d_in[4];
  const u16* Dv = (const u16*)d_in[5];
  const u16* wg = (const u16*)d_in[7];
  const u16* wu = (const u16*)d_in[8];
  const u16* wd = (const u16*)d_in[9];
  (void)hipFuncSetAttribute((const void*)cw_all, hipFuncAttributeMaxDynamicSharedMemorySize, K_LDS);
  cw_all<<<256, 512, K_LDS, stream>>>(x, wg, wu, (char*)d_ws, Kf, wd, k, v, sw, Dv, (u16*)d_out);
}
```
